```python
import torch

def _softmax(x, axis=-1):
    return torch.softmax(x, dim=axis)
def _gelu(x):
    return torch.nn.functional.gelu(x, approximate="tanh")
def _silu(x):
    return torch.nn.functional.silu(x)
def _sigmoid(x):
    return torch.sigmoid(x)
def _layernorm(x, axis=-1, eps=1e-5):
    m = x.mean(dim=axis, keepdim=True)
    v = x.var(dim=axis, keepdim=True, unbiased=False)
    return (x - m) / torch.sqrt(v + eps)
def _scatter_add(tgt, idx, src, axis=0):
    return tgt.clone().index_add_(axis, idx, src)
def _segment_sum(src, seg, n):
    out = torch.zeros((n,) + src.shape[1:], dtype=src.dtype)
    return out.index_add_(0, seg, src)
def _pad(x, pw):
    flat = tuple(v for pair in reversed(pw) for v in pair)
    return torch.nn.functional.pad(x, flat)

kernel_name = 'standalone__mha_block_cached__near_ridge'

def setup_inputs():

    _w = (lambda a: (lambda t: t.to(torch.bfloat16) if t.is_floating_point() else t)(torch.as_tensor(a)))
    B, Sq, D = 8, 256, 512
    H, Sk, Dh = 32, 1024, 128
    return {
        "x": _w((lambda *s: torch.randn(*s, dtype=torch.bfloat16) * 0.8)(B, Sq, D)),
        "k_cache": _w((lambda *s: torch.randn(*s, dtype=torch.bfloat16) * 0.8)(B, H, Sk, Dh)),
        "v_cache": _w((lambda *s: torch.randn(*s, dtype=torch.bfloat16) * 0.05)(B, H, Sk, Dh)),
        "wq": _w((lambda *s: torch.randn(*s, dtype=torch.bfloat16) * 0.8)(D, H * Dh) * (D ** -0.5)),
        "wo": _w((lambda *s: torch.randn(*s, dtype=torch.bfloat16) * 0.05)(H * Dh, D) * ((H * Dh) ** -0.5)),
    }

def reference(x, k_cache, v_cache, wq, wo):
    B, Sq, D = x.shape
    H, Dh = 32, 128
    q = (lambda x, *d: x.permute(*d))((x @ wq).reshape(B, Sq, H, Dh), 0, 2, 1, 3)
    scores = torch.einsum("bhqd,bhkd->bhqk", q, k_cache) * (Dh ** -0.5)
    p = _softmax(scores, axis=-1)
    o = torch.einsum("bhqk,bhkd->bhqd", p, v_cache)
    return (lambda x, *d: x.permute(*d))(o, 0, 2, 1, 3).reshape(B, Sq, H * Dh) @ wo
```

```cpp
#include <hip/hip_runtime.h>
#include <hip/hip_bf16.h>
typedef unsigned short u16;
__device__ inline float b2f(u16 v){ return __uint_as_float((unsigned)v<<16); }
__device__ inline u16 f2b(float f){ unsigned u=__float_as_uint(f); return (u16)((u + 0x7FFF + ((u>>16)&1))>>16); }
__global__ void qproj(const u16* x, const u16* wq, u16* q){
  int n = blockIdx.x*256+threadIdx.x; int m = blockIdx.y;
  float a=0; for(int k=0;k<512;k++) a += b2f(x[m*512+k])*b2f(wq[(size_t)k*4096+n]);
  q[(size_t)m*4096+n]=f2b(a);
}
__global__ void attn(const u16* q, const u16* K, const u16* V, u16* o){
  __shared__ float s[1024]; __shared__ float qs[128]; __shared__ float red[256];
  int row = blockIdx.x; int bh = blockIdx.y; int b = bh>>5, h = bh&31; int t=threadIdx.x;
  if(t<128) qs[t]=b2f(q[(size_t)(b*256+row)*4096+h*128+t]);
  __syncthreads();
  const u16* Kp=K+(size_t)bh*1024*128; const u16* Vp=V+(size_t)bh*1024*128;
  float mx=-1e30f;
  for(int j=t;j<1024;j+=256){ float a=0; for(int d=0;d<128;d++) a+=qs[d]*b2f(Kp[j*128+d]); a*=0.08838834764f; s[j]=a; mx=fmaxf(mx,a);}
  red[t]=mx; __syncthreads();
  for(int o2=128;o2>0;o2>>=1){ if(t<o2) red[t]=fmaxf(red[t],red[t+o2]); __syncthreads(); }
  mx=red[0]; __syncthreads();
  float sm=0; for(int j=t;j<1024;j+=256){ float e=__expf(s[j]-mx); s[j]=e; sm+=e; }
  red[t]=sm; __syncthreads();
  for(int o2=128;o2>0;o2>>=1){ if(t<o2) red[t]+=red[t+o2]; __syncthreads(); }
  sm=red[0];
  if(t<128){ float a=0; for(int j=0;j<1024;j++) a+=s[j]*b2f(Vp[j*128+t]); o[(size_t)(b*256+row)*4096+h*128+t]=f2b(a/sm);}
}
__global__ void oproj(const u16* o, const u16* wo, u16* out){
  int n = blockIdx.x*256+threadIdx.x; int m = blockIdx.y;
  float a=0; for(int k=0;k<4096;k++) a += b2f(o[(size_t)m*4096+k])*b2f(wo[(size_t)k*512+n]);
  out[(size_t)m*512+n]=f2b(a);
}
extern "C" void kernel_launch(void* const* d_in, const int* in_sizes, int n_in, void* d_out, int out_size, void* d_ws, size_t ws_size, hipStream_t stream){
  const u16* x=(const u16*)d_in[0]; const u16* K=(const u16*)d_in[1]; const u16* V=(const u16*)d_in[2]; const u16* wq=(const u16*)d_in[3]; const u16* wo=(const u16*)d_in[4];
  u16* q=(u16*)d_ws; u16* o=q+(size_t)2048*4096;
  qproj<<<dim3(16,2048),256,0,stream>>>(x,wq,q);
  attn<<<dim3(256,256),256,0,stream>>>(q,K,V,o);
  oproj<<<dim3(2,2048),256,0,stream>>>(o,wo,(u16*)d_out);
}
```

```cpp
#include <hip/hip_runtime.h>
typedef unsigned short u16;

__global__ __launch_bounds__(256, 1) void mha_main(const void* x, const void* kc, const void* vc, const void* wq, const void* wo, void* out, void* ws) {
  __shared__ __attribute__((aligned(16))) char lds[163840];
  const unsigned wid = __builtin_amdgcn_readfirstlane(threadIdx.x >> 6);
  asm volatile("" :: "s"((unsigned)(size_t)lds));
  asm volatile(
"s_load_dwordx8 s[4:11], %0, 0x0\n"
"s_load_dwordx4 s[12:15], %0, 0x20\n"
"s_load_dwordx2 s[16:17], %0, 0x30\n"
"s_mov_b32 s29, %2\n"
"s_and_b32 s68, %1, 7\n"
"s_lshr_b32 s69, %1, 3\n"
"s_and_b32 s35, s68, 1\n"
"s_sub_u32 s27, s68, s35\n"
"s_and_b32 s70, s69, 1\n"
"s_add_u32 s27, s27, s70\n"
"s_lshr_b32 s34, s69, 1\n"
"s_lshl_b32 s28, s35, 4\n"
"s_add_u32 s28, s28, s34\n"
"v_mbcnt_lo_u32_b32 v218, -1, 0\n"
"v_mbcnt_hi_u32_b32 v218, -1, v218\n"
"s_getpc_b64 s[76:77]\n"
"L_pc_%=:\n"
"s_lshl_b32 s69, s69, 2\n"
"s_add_u32 s69, s69, s29\n"
"s_lshl_b32 s69, s69, 8\n"
"v_lshl_add_u32 v250, v218, 2, s69\n"
"v_min_u32 v216, L_tail_%=-L_pc_%=, v250\n"
"global_load_dword a157, v216, s[76:77]\n"
"s_mul_i32 s31, s29, 1032\n"
"s_lshl_b32 s21, s29, 13\n"
"v_lshrrev_b32 v211, 4, v218\n"
"v_lshrrev_b32 v215, 3, v218\n"
"v_bfe_u32 v217, v218, 3, 2\n"
"v_and_b32 v214, 7, v218\n"
"v_lshrrev_b32 v213, 5, v218\n"
"v_and_b32 v212, 1, v211\n"
"v_lshlrev_b32 v210, 2, v212\n"
"v_xor_b32 v210, v210, v214\n"
"v_lshlrev_b32 v210, 4, v210\n"
"v_lshl_add_u32 v210, v213, 7, v210\n"
"s_lshl_b32 s68, s29, 16\n"
"v_lshl_add_u32 v200, v217, 13, v210\n"
"v_add_u32 v200, s68, v200\n"
"v_add_u32 v201, 0x8000, v200\n"
"v_xor_b32 v210, v214, v211\n"
"v_lshlrev_b32 v210, 4, v210\n"
"v_lshl_add_u32 v192, v215, 10, v210\n"
"v_add_u32 v192, s68, v192\n"
"v_xor_b32 v193, 64, v192\n"
"v_add_u32 v193, 0x2000, v193\n"
"s_xor_b32 s68, s35, 1\n"
"s_lshl_b32 s68, s68, 15\n"
"v_xor_b32 v192, s68, v192\n"
"v_xor_b32 v193, s68, v193\n"
"v_xor_b32 v194, 16384, v192\n"
"v_xor_b32 v195, 16384, v193\n"
"v_xor_b32 v196, 32768, v192\n"
"v_xor_b32 v197, 32768, v193\n"
"v_xor_b32 v198, 49152, v192\n"
"v_xor_b32 v199, 49152, v193\n"
"s_waitcnt lgkmcnt(0)\n"
"s_memrealtime s[82:83]\n"
"s_lshl_b32 s68, s28, 8\n"
"s_mov_b64 s[40:41], s[10:11]\n"
"s_add_u32 s40, s40, s68\n"
"s_addc_u32 s41, s41, 0\n"
"s_and_b32 s41, s41, 0xffff\n"
"s_mov_b32 s42, 0x400000\n"
"s_mov_b32 s43, 0x20000\n"
"s_sub_u32 s42, s42, s68\n"
"s_lshl_b32 s68, s27, 18\n"
"s_mov_b64 s[36:37], s[4:5]\n"
"s_add_u32 s36, s36, s68\n"
"s_addc_u32 s37, s37, 0\n"
"s_and_b32 s37, s37, 0xffff\n"
"s_mov_b32 s38, 0x40000\n"
"s_mov_b32 s39, 0x20000\n"
"s_mov_b32 s22, 0\n"
"s_mov_b32 s23, 0\n"
"s_mov_b32 s24, 262144\n"
"s_add_u32 m0, s31, 82048\n"
"s_nop 0\n"
"buffer_load_dwordx4 v200, s[40:43], s23 offen lds\n"
"s_add_u32 m0, s31, 86176\n"
"s_lshl_b32 s20, s29, 10\n"
"s_lshl_b32 s33, s29, 12\n"
"s_lshl_b32 s68, s29, 1\n"
"v_add_u32 v216, s68, v212\n"
"v_lshlrev_b32 v213, 7, v213\n"
"v_lshl_add_u32 v213, v217, 8, v213\n"
"v_add_u32 v213, s20, v213\n"
"v_xor_b32 v210, v214, v216\n"
"v_lshl_add_u32 v246, v210, 4, v213\n"
"v_lshlrev_b32 v210, 1, v216\n"
"buffer_load_dwordx4 v201, s[40:43], s23 offen lds\n"
"s_add_u32 m0, s31, 90304\n"
"v_xor_b32 v210, v210, v214\n"
"v_and_b32 v210, 7, v210\n"
"v_lshl_add_u32 v247, v210, 4, v213\n"
"v_lshlrev_b32 v210, 1, v211\n"
"v_xor_b32 v210, v210, v214\n"
"v_lshlrev_b32 v210, 4, v210\n"
"v_lshl_add_u32 v248, v215, 10, v210\n"
"v_and_b32 v212, 15, v218\n"
"v_lshrrev_b32 v213, 1, v212\n"
"v_xor_b32 v213, v213, v211\n"
"buffer_load_dwordx4 v200, s[40:43], s24 offen lds\n"
"s_add_u32 m0, s31, 94432\n"
"v_lshlrev_b32 v213, 4, v213\n"
"v_lshl_add_u32 v202, v212, 7, v213\n"
"v_xor_b32 v203, 64, v202\n"
"v_add_u32 v202, s21, v202\n"
"v_add_u32 v203, s21, v203\n"
"v_add_u32 v204, 0x20000, v202\n"
"v_add_u32 v205, 0x20000, v203\n"
"v_and_b32 v216, 3, v218\n"
"v_lshl_add_u32 v213, v216, 7, v213\n"
"v_lshrrev_b32 v212, 2, v212\n"
"buffer_load_dwordx4 v201, s[40:43], s24 offen lds\n"
"s_add_u32 m0, s21, 32768\n"
"v_lshl_add_u32 v240, v212, 10, v213\n"
"v_xor_b32 v241, 64, v240\n"
"v_lshlrev_b32 v213, 5, v217\n"
"v_lshl_add_u32 v213, v216, 3, v213\n"
"v_lshl_add_u32 v213, v212, 7, v213\n"
"v_lshl_add_u32 v242, v211, 10, v213\n"
"v_add_u32 v242, 16384, v242\n"
"v_lshl_add_u32 v249, v211, 9, v213\n"
"v_add_u32 v249, s33, v249\n"
"v_xor_b32 v243, 32, v242\n"
"buffer_load_dwordx4 v192, s[36:39], s22 offen lds\n"
"s_add_u32 m0, s21, 33792\n"
"v_xor_b32 v244, 64, v242\n"
"v_xor_b32 v245, 96, v242\n"
"v_lshrrev_b32 v210, 1, v212\n"
"v_lshlrev_b32 v210, 6, v210\n"
"v_lshl_add_u32 v210, v212, 7, v210\n"
"v_lshl_add_u32 v210, v216, 4, v210\n"
"v_xor_b32 v213, 64, v210\n"
"v_mul_u32_u24 v212, 1032, v211\n"
"v_add_u32 v212, 65536, v212\n"
"v_add_u32 v206, v212, v210\n"
"buffer_load_dwordx4 v193, s[36:39], s22 offen lds\n"
"s_add_u32 m0, s21, 34816\n"
"v_add_u32 v207, v212, v213\n"
"s_lshl_b32 s68, s27, 5\n"
"s_add_u32 s71, s68, s28\n"
"s_lshl_b32 s68, s71, 18\n"
"s_mov_b64 s[44:45], s[6:7]\n"
"s_add_u32 s44, s44, s68\n"
"s_addc_u32 s45, s45, 0\n"
"s_and_b32 s45, s45, 0xffff\n"
"s_mov_b32 s46, 0x40000\n"
"s_mov_b32 s47, 0x20000\n"
"buffer_load_dwordx4 v194, s[36:39], s22 offen lds\n"
"s_add_u32 m0, s21, 35840\n"
"s_mov_b64 s[48:49], s[8:9]\n"
"s_add_u32 s48, s48, s68\n"
"s_addc_u32 s49, s49, 0\n"
"s_and_b32 s49, s49, 0xffff\n"
"s_mov_b32 s50, 0x40000\n"
"s_mov_b32 s51, 0x20000\n"
"s_lshl_b32 s68, s27, 21\n"
"s_mov_b64 s[52:53], s[16:17]\n"
"s_add_u32 s52, s52, s68\n"
"s_addc_u32 s53, s53, 0\n"
"buffer_load_dwordx4 v195, s[36:39], s22 offen lds\n"
"s_add_u32 m0, s21, 36864\n"
"s_and_b32 s53, s53, 0xffff\n"
"s_mov_b32 s54, 0x200000\n"
"s_mov_b32 s55, 0x20000\n"
"s_lshl_b32 s68, s27, 15\n"
"s_mov_b64 s[60:61], s[16:17]\n"
"s_add_u32 s68, s68, 0x2800000\n"
"s_add_u32 s60, s60, s68\n"
"s_addc_u32 s61, s61, 0\n"
"s_and_b32 s61, s61, 0xffff\n"
"s_mov_b32 s62, 0x8000\n"
"buffer_load_dwordx4 v196, s[36:39], s22 offen lds\n"
"s_add_u32 m0, s21, 37888\n"
"s_mov_b32 s63, 0x20000\n"
"s_lshl_b32 s68, s71, 2\n"
"s_add_u32 s68, s68, s29\n"
"s_lshl_b32 s68, s68, 14\n"
"s_mov_b64 s[64:65], s[16:17]\n"
"s_add_u32 s68, s68, 0x1000000\n"
"s_add_u32 s64, s64, s68\n"
"s_addc_u32 s65, s65, 0\n"
"s_and_b32 s65, s65, 0xffff\n"
"s_mov_b32 s66, 0x4000\n"
"buffer_load_dwordx4 v197, s[36:39], s22 offen lds\n"
"s_add_u32 m0, s21, 38912\n"
"s_mov_b32 s67, 0x20000\n"
"s_lshl_b32 s68, s35, 2\n"
"s_add_u32 s68, s68, s29\n"
"s_lshl_b32 s68, s68, 19\n"
"s_lshr_b32 s69, s34, 1\n"
"s_lshl_b32 s69, s69, 7\n"
"s_add_u32 s68, s68, s69\n"
"s_mov_b64 s[56:57], s[12:13]\n"
"s_add_u32 s56, s56, s68\n"
"s_addc_u32 s57, s57, 0\n"
"buffer_load_dwordx4 v198, s[36:39], s22 offen lds\n"
"s_add_u32 m0, s21, 39936\n"
"s_and_b32 s57, s57, 0xffff\n"
"s_mov_b32 s58, 0x80000\n"
"s_mov_b32 s59, 0x20000\n"
"s_sub_u32 s58, s58, s69\n"
"s_and_b32 s80, s27, 1\n"
"s_lshl_b32 s80, s80, 18\n"
"s_xor_b32 s81, s80, 0x40000\n"
"s_lshl_b32 s74, s28, 2\n"
"s_lshl_b32 s75, s28, 10\n"
"v_lshlrev_b32 v251, 4, v218\n"
"buffer_load_dwordx4 v199, s[36:39], s22 offen lds\n"
"s_waitcnt lgkmcnt(0)\n"
"s_add_u32 s18, s82, 770\n"
"s_mov_b32 s19, s82\n"
"s_mov_b32 s22, 128\n"
"s_mov_b32 s23, 524288\n"
"s_mov_b32 s24, 786432\n"
"s_add_u32 m0, s31, 98560\n"
"v_bfe_u32 v210, v218, 1, 1\n"
"v_lshrrev_b32 v212, 5, v218\n"
"v_cmp_eq_u32 vcc, v210, v212\n"
"v_mov_b32 v213, 0x3f803f80\n"
"s_nop 0\n"
"v_cndmask_b32 v212, 0, v213, vcc\n"
"v_and_b32 v210, 1, v218\n"
"buffer_load_dwordx4 v200, s[40:43], s23 offen lds\n"
"s_add_u32 m0, s31, 102688\n"
"v_mul_u32_u24 v210, 0xaaaa, v210\n"
"v_add_u32 v239, 0x4444, v210\n"
"v_accvgpr_write_b32 a144, v212\n"
"v_accvgpr_write_b32 a148, 0\n"
"v_accvgpr_write_b32 a145, v212\n"
"v_accvgpr_write_b32 a149, 0\n"
"buffer_load_dwordx4 v201, s[40:43], s23 offen lds\n"
"s_add_u32 m0, s31, 106816\n"
"v_accvgpr_write_b32 a146, v212\n"
"v_accvgpr_write_b32 a150, 0\n"
"v_accvgpr_write_b32 a147, v212\n"
"v_accvgpr_write_b32 a151, 0\n"
"v_accvgpr_write_b32 a156, 1\n"
"v_accvgpr_write_b32 a96, 0\n"
"buffer_load_dwordx4 v200, s[40:43], s24 offen lds\n"
"s_add_u32 m0, s31, 110944\n"
"v_accvgpr_write_b32 a97, 0\n"
"v_accvgpr_write_b32 a98, 0\n"
"v_accvgpr_write_b32 a99, 0\n"
"v_accvgpr_write_b32 a100, 0\n"
"v_accvgpr_write_b32 a101, 0\n"
"v_accvgpr_write_b32 a102, 0\n"
"buffer_load_dwordx4 v201, s[40:43], s24 offen lds\n"
"s_add_u32 m0, s21, 131072\n"
"v_accvgpr_write_b32 a103, 0\n"
"v_accvgpr_write_b32 a104, 0\n"
"v_accvgpr_write_b32 a105, 0\n"
"v_accvgpr_write_b32 a106, 0\n"
"v_accvgpr_write_b32 a107, 0\n"
"v_accvgpr_write_b32 a108, 0\n"
"buffer_load_dwordx4 v192, s[36:39], s22 offen lds\n"
"s_add_u32 m0, s21, 132096\n"
"v_accvgpr_write_b32 a109, 0\n"
"v_accvgpr_write_b32 a110, 0\n"
"v_accvgpr_write_b32 a111, 0\n"
"v_accvgpr_write_b32 a112, 0\n"
"v_accvgpr_write_b32 a113, 0\n"
"v_accvgpr_write_b32 a114, 0\n"
"buffer_load_dwordx4 v193, s[36:39], s22 offen lds\n"
"s_add_u32 m0, s21, 133120\n"
"v_accvgpr_write_b32 a115, 0\n"
"v_accvgpr_write_b32 a116, 0\n"
"v_accvgpr_write_b32 a117, 0\n"
"v_accvgpr_write_b32 a118, 0\n"
"v_accvgpr_write_b32 a119, 0\n"
"v_accvgpr_write_b32 a120, 0\n"
"buffer_load_dwordx4 v194, s[36:39], s22 offen lds\n"
"s_add_u32 m0, s21, 134144\n"
"v_accvgpr_write_b32 a121, 0\n"
"v_accvgpr_write_b32 a122, 0\n"
"v_accvgpr_write_b32 a123, 0\n"
"v_accvgpr_write_b32 a124, 0\n"
"v_accvgpr_write_b32 a125, 0\n"
"v_accvgpr_write_b32 a126, 0\n"
"buffer_load_dwordx4 v195, s[36:39], s22 offen lds\n"
"s_add_u32 m0, s21, 135168\n"
"v_accvgpr_write_b32 a127, 0\n"
"v_accvgpr_write_b32 a128, 0\n"
"v_accvgpr_write_b32 a129, 0\n"
"v_accvgpr_write_b32 a130, 0\n"
"v_accvgpr_write_b32 a131, 0\n"
"v_accvgpr_write_b32 a132, 0\n"
"buffer_load_dwordx4 v196, s[36:39], s22 offen lds\n"
"s_add_u32 m0, s21, 136192\n"
"v_accvgpr_write_b32 a133, 0\n"
"v_accvgpr_write_b32 a134, 0\n"
"v_accvgpr_write_b32 a135, 0\n"
"v_accvgpr_write_b32 a136, 0\n"
"v_accvgpr_write_b32 a137, 0\n"
"v_accvgpr_write_b32 a138, 0\n"
"buffer_load_dwordx4 v197, s[36:39], s22 offen lds\n"
"s_add_u32 m0, s21, 137216\n"
"v_accvgpr_write_b32 a139, 0\n"
"v_accvgpr_write_b32 a140, 0\n"
"v_accvgpr_write_b32 a141, 0\n"
"v_accvgpr_write_b32 a142, 0\n"
"v_accvgpr_write_b32 a143, 0\n"
"s_lshl_b32 s68, s35, 4\n"
"buffer_load_dwordx4 v198, s[36:39], s22 offen lds\n"
"s_add_u32 m0, s21, 138240\n"
"v_add_u32 v212, s68, v218\n"
"v_min_u32 v213, 16, v218\n"
"v_subrev_u32 v213, 15, v213\n"
"v_lshlrev_b32 v213, 4, v213\n"
"v_add_u32 v213, s28, v213\n"
"v_and_b32 v213, 31, v213\n"
"buffer_load_dwordx4 v199, s[36:39], s22 offen lds\n"
"s_mov_b32 s22, 256\n"
"s_mov_b32 s23, 1048576\n"
"s_mov_b32 s24, 1310720\n"
"s_waitcnt vmcnt(12)\n"
"s_barrier\n"
"ds_read_b128 a[192:195], v202 offset:32768\n"
"ds_read_b128 a[196:199], v202 offset:34816\n"
"ds_read_b128 a[200:203], v202 offset:36864\n"
"ds_read_b128 a[204:207], v202 offset:38912\n"
"ds_read_b64_tr_b16 a[160:161], v206 offset:16512\n"
"ds_read_b64_tr_b16 a[162:163], v206 offset:20640\n"
"ds_read_b64_tr_b16 a[164:165], v206 offset:16520\n"
"ds_read_b64_tr_b16 a[166:167], v206 offset:20648\n"
"ds_read_b64_tr_b16 a[168:169], v207 offset:16512\n"
"ds_read_b64_tr_b16 a[170:171], v207 offset:20640\n"
"s_waitcnt lgkmcnt(4)\n"
"v_mfma_f32_16x16x32_bf16 v[64:67], a[160:163], a[192:195], 0\n"
"ds_read_b64_tr_b16 a[172:173], v207 offset:16520\n"
"v_mfma_f32_16x16x32_bf16 v[68:71], a[160:163], a[196:199], 0\n"
"v_mfma_f32_16x16x32_bf16 v[72:75], a[160:163], a[200:203], 0\n"
"ds_read_b64_tr_b16 a[174:175], v207 offset:20648\n"
"v_mfma_f32_16x16x32_bf16 v[76:79], a[160:163], a[204:207], 0\n"
"s_waitcnt lgkmcnt(4)\n"
"s_add_u32 m0, s31, 65536\n"
"ds_read_b64_tr_b16 a[176:177], v206 offset:17024\n"
"v_mfma_f32_16x16x32_bf16 v[92:95], a[164:167], a[204:207], 0\n"
"buffer_load_dwordx4 v200, s[40:43], s23 offen lds\n"
"v_mfma_f32_16x16x32_bf16 v[88:91], a[164:167], a[200:203], 0\n"
"ds_read_b64_tr_b16 a[178:179], v206 offset:21152\n"
"v_mfma_f32_16x16x32_bf16 v[84:87], a[164:167], a[196:199], 0\n"
"s_add_u32 m0, s31, 69664\n"
"ds_read_b128 a[208:211], v203 offset:32768\n"
"v_mfma_f32_16x16x32_bf16 v[80:83], a[164:167], a[192:195], 0\n"
"s_waitcnt lgkmcnt(5)\n"
"buffer_load_dwordx4 v201, s[40:43], s23 offen lds\n"
"v_mfma_f32_16x16x32_bf16 v[96:99], a[168:171], a[192:195], 0\n"
"ds_read_b64_tr_b16 a[180:181], v206 offset:17032\n"
"v_mfma_f32_16x16x32_bf16 v[100:103], a[168:171], a[196:199], 0\n"
"v_mfma_f32_16x16x32_bf16 v[104:107], a[168:171], a[200:203], 0\n"
"ds_read_b64_tr_b16 a[182:183], v206 offset:21160\n"
"v_mfma_f32_16x16x32_bf16 v[108:111], a[168:171], a[204:207], 0\n"
"s_waitcnt lgkmcnt(5)\n"
"s_add_u32 m0, s31, 73792\n"
"ds_read_b128 a[212:215], v203 offset:34816\n"
"v_mfma_f32_16x16x32_bf16 v[124:127], a[172:175], a[204:207], 0\n"
"buffer_load_dwordx4 v200, s[40:43], s24 offen lds\n"
"v_mfma_f32_16x16x32_bf16 v[120:123], a[172:175], a[200:203], 0\n"
"ds_read_b64_tr_b16 a[184:185], v207 offset:17024\n"
"v_mfma_f32_16x16x32_bf16 v[116:119], a[172:175], a[196:199], 0\n"
"s_add_u32 m0, s31, 77920\n"
"ds_read_b64_tr_b16 a[186:187], v207 offset:21152\n"
"v_mfma_f32_16x16x32_bf16 v[112:115], a[172:175], a[192:195], 0\n"
"s_waitcnt lgkmcnt(6)\n"
"buffer_load_dwordx4 v201, s[40:43], s24 offen lds\n"
"v_mfma_f32_16x16x32_bf16 v[128:131], a[176:179], a[192:195], 0\n"
"ds_read_b128 a[216:219], v203 offset:36864\n"
"v_mfma_f32_16x16x32_bf16 v[132:135], a[176:179], a[196:199], 0\n"
"v_mfma_f32_16x16x32_bf16 v[136:139], a[176:179], a[200:203], 0\n"
"ds_read_b64_tr_b16 a[188:189], v207 offset:17032\n"
"v_mfma_f32_16x16x32_bf16 v[140:143], a[176:179], a[204:207], 0\n"
"s_waitcnt lgkmcnt(5)\n"
"s_add_u32 m0, s21, 0\n"
"ds_read_b64_tr_b16 a[190:191], v207 offset:21160\n"
"v_mfma_f32_16x16x32_bf16 v[156:159], a[180:183], a[204:207], 0\n"
"buffer_load_dwordx4 v192, s[36:39], s22 offen lds\n"
"v_mfma_f32_16x16x32_bf16 v[152:155], a[180:183], a[200:203], 0\n"
"ds_read_b128 a[220:223], v203 offset:38912\n"
"v_mfma_f32_16x16x32_bf16 v[148:151], a[180:183], a[196:199], 0\n"
"s_add_u32 m0, s21, 1024\n"
"ds_read_b64_tr_b16 a[160:161], v206 offset:24768\n"
"v_mfma_f32_16x16x32_bf16 v[144:147], a[180:183], a[192:195], 0\n"
"s_waitcnt lgkmcnt(5)\n"
"buffer_load_dwordx4 v193, s[36:39], s22 offen lds\n"
"v_mfma_f32_16x16x32_bf16 v[160:163], a[184:187], a[192:195], 0\n"
"ds_read_b64_tr_b16 a[162:163], v206 offset:28896\n"
"v_mfma_f32_16x16x32_bf16 v[164:167], a[184:187], a[196:199], 0\n"
"v_mfma_f32_16x16x32_bf16 v[168:171], a[184:187], a[200:203], 0\n"
"ds_read_b64_tr_b16 a[164:165], v206 offset:24776\n"
"v_mfma_f32_16x16x32_bf16 v[172:175], a[184:187], a[204:207], 0\n"
"s_waitcnt lgkmcnt(4)\n"
"s_add_u32 m0, s21, 2048\n"
"ds_read_b64_tr_b16 a[166:167], v206 offset:28904\n"
"v_mfma_f32_16x16x32_bf16 v[188:191], a[188:191], a[204:207], 0\n"
"buffer_load_dwordx4 v194, s[36:39], s22 offen lds\n"
"v_mfma_f32_16x16x32_bf16 v[184:187], a[188:191], a[200:203], 0\n"
"ds_read_b64_tr_b16 a[168:169], v207 offset:24768\n"
"v_mfma_f32_16x16x32_bf16 v[180:183], a[188:191], a[196:199], 0\n"
"s_add_u32 m0, s21, 3072\n"
"ds_read_b64_tr_b16 a[170:171], v207 offset:28896\n"
"v_mfma_f32_16x16x32_bf16 v[176:179], a[188:191], a[192:195], 0\n"
"s_waitcnt lgkmcnt(4)\n"
"buffer_load_dwordx4 v195, s[36:39], s22 offen lds\n"
"v_mfma_f32_16x16x32_bf16 v[64:67], a[160:163], a[208:211], v[64:67]\n"
"ds_read_b64_tr_b16 a[172:173], v207 offset:24776\n"
"v_mfma_f32_16x16x32_bf16 v[68:71], a[160:163], a[212:215], v[68:71]\n"
"v_mfma_f32_16x16x32_bf16 v[72:75], a[160:163], a[216:219], v[72:75]\n"
"ds_read_b64_tr_b16 a[174:175], v207 offset:28904\n"
"v_mfma_f32_16x16x32_bf16 v[76:79], a[160:163], a[220:223], v[76:79]\n"
"s_waitcnt lgkmcnt(4)\n"
"s_add_u32 m0, s21, 4096\n"
"ds_read_b64_tr_b16 a[176:177], v206 offset:25280\n"
"v_mfma_f32_16x16x32_bf16 v[92:95], a[164:167], a[220:223], v[92:95]\n"
"buffer_load_dwordx4 v196, s[36:39], s22 offen lds\n"
"v_mfma_f32_16x16x32_bf16 v[88:91], a[164:167], a[216:219], v[88:91]\n"
"ds_read_b64_tr_b16 a[178:179], v206 offset:29408\n"
"v_mfma_f32_16x16x32_bf16 v[84:87], a[164:167], a[212:215], v[84:87]\n"
"s_add_u32 m0, s21, 5120\n"
"ds_read_b64_tr_b16 a[180:181], v206 offset:25288\n"
"v_mfma_f32_16x16x32_bf16 v[80:83], a[164:167], a[208:211], v[80:83]\n"
"s_waitcnt lgkmcnt(5)\n"
"buffer_load_dwordx4 v197, s[36:39], s22 offen lds\n"
"v_mfma_f32_16x16x32_bf16 v[96:99], a[168:171], a[208:211], v[96:99]\n"
"ds_read_b64_tr_b16 a[182:183], v206 offset:29416\n"
"v_mfma_f32_16x16x32_bf16 v[100:103], a[168:171], a[212:215], v[100:103]\n"
"v_mfma_f32_16x16x32_bf16 v[104:107], a[168:171], a[216:219], v[104:107]\n"
"ds_read_b64_tr_b16 a[184:185], v207 offset:25280\n"
"v_mfma_f32_16x16x32_bf16 v[108:111], a[168:171], a[220:223], v[108:111]\n"
"s_waitcnt lgkmcnt(5)\n"
"s_add_u32 m0, s21, 6144\n"
"ds_read_b64_tr_b16 a[186:187], v207 offset:29408\n"
"v_mfma_f32_16x16x32_bf16 v[124:127], a[172:175], a[220:223], v[124:127]\n"
"buffer_load_dwordx4 v198, s[36:39], s22 offen lds\n"
"v_mfma_f32_16x16x32_bf16 v[120:123], a[172:175], a[216:219], v[120:123]\n"
"ds_read_b64_tr_b16 a[188:189], v207 offset:25288\n"
"v_mfma_f32_16x16x32_bf16 v[116:119], a[172:175], a[212:215], v[116:119]\n"
"s_add_u32 m0, s21, 7168\n"
"ds_read_b64_tr_b16 a[190:191], v207 offset:29416\n"
"v_mfma_f32_16x16x32_bf16 v[112:115], a[172:175], a[208:211], v[112:115]\n"
"s_waitcnt lgkmcnt(6)\n"
"buffer_load_dwordx4 v199, s[36:39], s22 offen lds\n"
"v_mfma_f32_16x16x32_bf16 v[128:131], a[176:179], a[208:211], v[128:131]\n"
"v_mfma_f32_16x16x32_bf16 v[132:135], a[176:179], a[212:215], v[132:135]\n"
"v_mfma_f32_16x16x32_bf16 v[136:139], a[176:179], a[216:219], v[136:139]\n"
"v_mfma_f32_16x16x32_bf16 v[140:143], a[176:179], a[220:223], v[140:143]\n"
"s_waitcnt lgkmcnt(4)\n"
"v_mfma_f32_16x16x32_bf16 v[156:159], a[180:183], a[220:223], v[156:159]\n"
"s_waitcnt vmcnt(12) lgkmcnt(0)\n"
"s_barrier\n"
"v_mfma_f32_16x16x32_bf16 v[152:155], a[180:183], a[216:219], v[152:155]\n"
"ds_read_b128 a[192:195], v204 offset:0\n"
"v_mfma_f32_16x16x32_bf16 v[148:151], a[180:183], a[212:215], v[148:151]\n"
"ds_read_b64_tr_b16 a[160:161], v206 offset:33024\n"
"v_mfma_f32_16x16x32_bf16 v[144:147], a[180:183], a[208:211], v[144:147]\n"
"ds_read_b64_tr_b16 a[162:163], v206 offset:37152\n"
"v_mfma_f32_16x16x32_bf16 v[160:163], a[184:187], a[208:211], v[160:163]\n"
"ds_read_b128 a[196:199], v204 offset:2048\n"
"v_mfma_f32_16x16x32_bf16 v[164:167], a[184:187], a[212:215], v[164:167]\n"
"ds_read_b128 a[200:203], v204 offset:4096\n"
"v_mfma_f32_16x16x32_bf16 v[168:171], a[184:187], a[216:219], v[168:171]\n"
"ds_read_b128 a[204:207], v204 offset:6144\n"
"v_mfma_f32_16x16x32_bf16 v[172:175], a[184:187], a[220:223], v[172:175]\n"
"ds_read_b64_tr_b16 a[164:165], v206 offset:33032\n"
"v_mfma_f32_16x16x32_bf16 v[188:191], a[188:191], a[220:223], v[188:191]\n"
"s_mov_b32 s22, 384\n"
"ds_read_b64_tr_b16 a[166:167], v206 offset:37160\n"
"v_mfma_f32_16x16x32_bf16 v[184:187], a[188:191], a[216:219], v[184:187]\n"
"s_mov_b32 s23, 1572864\n"
"ds_read_b64_tr_b16 a[168:169], v207 offset:33024\n"
"v_mfma_f32_16x16x32_bf16 v[180:183], a[188:191], a[212:215], v[180:183]\n"
"s_mov_b32 s24, 1835008\n"
"ds_read_b64_tr_b16 a[170:171], v207 offset:37152\n"
"v_mfma_f32_16x16x32_bf16 v[176:179], a[188:191], a[208:211], v[176:179]\n"
"s_waitcnt lgkmcnt(7)\n"
"v_mfma_f32_16x16x32_bf16 v[64:67], a[160:163], a[192:195], v[64:67]\n"
"s_waitcnt lgkmcnt(6)\n"
"ds_read_b64_tr_b16 a[172:173], v207 offset:33032\n"
"v_cmp_gt_u32 vcc, 16, v218\n"
"v_mfma_f32_16x16x32_bf16 v[68:71], a[160:163], a[196:199], v[68:71]\n"
"s_waitcnt lgkmcnt(6)\n"
"v_cndmask_b32 v212, v213, v212, vcc\n"
"v_lshlrev_b32 v254, 2, v212\n"
"v_mfma_f32_16x16x32_bf16 v[72:75], a[160:163], a[200:203], v[72:75]\n"
"s_waitcnt lgkmcnt(5)\n"
"ds_read_b64_tr_b16 a[174:175], v207 offset:37160\n"
"v_lshlrev_b32 v212, 10, v212\n"
"v_mfma_f32_16x16x32_bf16 v[76:79], a[160:163], a[204:207], v[76:79]\n"
"s_waitcnt lgkmcnt(4)\n"
"s_add_u32 m0, s31, 82048\n"
"ds_read_b64_tr_b16 a[176:177], v206 offset:33536\n"
"v_mfma_f32_16x16x32_bf16 v[92:95], a[164:167], a[204:207], v[92:95]\n"
"s_lshl_b32 s68, s29, 6\n"
"buffer_load_dwordx4 v200, s[40:43], s23 offen lds\n"
"v_mfma_f32_16x16x32_bf16 v[88:91], a[164:167], a[200:203], v[88:91]\n"
"ds_read_b64_tr_b16 a[178:179], v206 offset:37664\n"
"v_add_u32 v213, s68, v218\n"
"v_mfma_f32_16x16x32_bf16 v[84:87], a[164:167], a[196:199], v[84:87]\n"
"s_add_u32 m0, s31, 86176\n"
"ds_read_b128 a[208:211], v205 offset:0\n"
"v_cmp_gt_u32 vcc, 17, v213\n"
"v_mfma_f32_16x16x32_bf16 v[80:83], a[164:167], a[192:195], v[80:83]\n"
"s_waitcnt lgkmcnt(5)\n"
"buffer_load_dwordx4 v201, s[40:43], s23 offen lds\n"
"v_mfma_f32_16x16x32_bf16 v[96:99], a[168:171], a[192:195], v[96:99]\n"
"ds_read_b64_tr_b16 a[180:181], v206 offset:33544\n"
"v_mov_b32 v213, 0x40000000\n"
"v_mfma_f32_16x16x32_bf16 v[100:103], a[168:171], a[196:199], v[100:103]\n"
"v_cndmask_b32 v253, v213, v212, vcc\n"
"v_mfma_f32_16x16x32_bf16 v[104:107], a[168:171], a[200:203], v[104:107]\n"
"s_lshl_b32 s68, s29, 28\n"
"ds_read_b64_tr_b16 a[182:183], v206 offset:37672\n"
"v_add_u32 v208, s68, v251\n"
"v_mfma_f32_16x16x32_bf16 v[108:111], a[168:171], a[204:207], v[108:111]\n"
"s_waitcnt lgkmcnt(5)\n"
"s_add_u32 m0, s31, 90304\n"
"ds_read_b128 a[212:215], v205 offset:2048\n"
"v_mfma_f32_16x16x32_bf16 v[124:127], a[172:175], a[204:207], v[124:127]\n"
"s_mov_b32 s30, 0x3e0293ee\n"
"buffer_load_dwordx4 v200, s[40:43], s24 offen lds\n"
"v_mfma_f32_16x16x32_bf16 v[120:123], a[172:175], a[200:203], v[120:123]\n"
"s_mov_b32 s25, 0\n"
"ds_read_b64_tr_b16 a[184:185], v207 offset:33536\n"
"v_mfma_f32_16x16x32_bf16 v[116:119], a[172:175], a[196:199], v[116:119]\n"
"s_add_u32 m0, s31, 94432\n"
"ds_read_b64_tr_b16 a[186:187], v207 offset:37664\n"
"v_mfma_f32_16x16x32_bf16 v[112:115], a[172:175], a[192:195], v[112:115]\n"
"s_waitcnt lgkmcnt(6)\n"
"buffer_load_dwordx4 v201, s[40:43], s24 offen lds\n"
"v_mfma_f32_16x16x32_bf16 v[128:131], a[176:179], a[192:195], v[128:131]\n"
"s_mov_b32 s26, 0\n"
"ds_read_b128 a[216:219], v205 offset:4096\n"
"v_mfma_f32_16x16x32_bf16 v[132:135], a[176:179], a[196:199], v[132:135]\n"
"s_mov_b32 s78, 0\n"
"v_mfma_f32_16x16x32_bf16 v[136:139], a[176:179], a[200:203], v[136:139]\n"
"s_lshl_b32 s68, %1, 14\n"
"ds_read_b64_tr_b16 a[188:189], v207 offset:33544\n"
"v_mfma_f32_16x16x32_bf16 v[140:143], a[176:179], a[204:207], v[140:143]\n"
"s_waitcnt lgkmcnt(5)\n"
"s_add_u32 m0, s21, 32768\n"
"ds_read_b64_tr_b16 a[190:191], v207 offset:37672\n"
"v_mfma_f32_16x16x32_bf16 v[156:159], a[180:183], a[204:207], v[156:159]\n"
"s_add_u32 s68, s68, s33\n"
"buffer_load_dwordx4 v192, s[36:39], s22 offen lds\n"
"v_mfma_f32_16x16x32_bf16 v[152:155], a[180:183], a[200:203], v[152:155]\n"
"s_min_u32 s68, s68, 4188928\n"
"ds_read_b128 a[220:223], v205 offset:6144\n"
"v_lshrrev_b32 v210, 2, v218\n"
"v_mfma_f32_16x16x32_bf16 v[148:151], a[180:183], a[196:199], v[148:151]\n"
"s_add_u32 m0, s21, 33792\n"
"ds_read_b64_tr_b16 a[160:161], v206 offset:41280\n"
"v_min_u32 v210, 2, v210\n"
"v_mfma_f32_16x16x32_bf16 v[144:147], a[180:183], a[192:195], v[144:147]\n"
"s_waitcnt lgkmcnt(5)\n"
"buffer_load_dwordx4 v193, s[36:39], s22 offen lds\n"
"v_mfma_f32_16x16x32_bf16 v[160:163], a[184:187], a[192:195], v[160:163]\n"
"ds_read_b64_tr_b16 a[162:163], v206 offset:45408\n"
"v_lshl_add_u32 v210, v210, 7, s68\n"
"v_mfma_f32_16x16x32_bf16 v[164:167], a[184:187], a[196:199], v[164:167]\n"
"v_and_b32 v212, 0xffffff00, v250\n"
"v_bfe_u32 v213, v218, 4, 1\n"
"v_mfma_f32_16x16x32_bf16 v[168:171], a[184:187], a[200:203], v[168:171]\n"
"ds_read_b64_tr_b16 a[164:165], v206 offset:41288\n"
"v_lshl_add_u32 v212, v213, 7, v212\n"
"v_mfma_f32_16x16x32_bf16 v[172:175], a[184:187], a[204:207], v[172:175]\n"
"s_waitcnt lgkmcnt(4)\n"
"s_add_u32 m0, s21, 34816\n"
"ds_read_b64_tr_b16 a[166:167], v206 offset:45416\n"
"v_add_u32 v212, L_tail_%=-L_pc_%=, v212\n"
"v_mfma_f32_16x16x32_bf16 v[188:191], a[188:191], a[204:207], v[188:191]\n"
"buffer_load_dwordx4 v194, s[36:39], s22 offen lds\n"
"v_mfma_f32_16x16x32_bf16 v[184:187], a[188:191], a[200:203], v[184:187]\n"
"ds_read_b64_tr_b16 a[168:169], v207 offset:41280\n"
"v_min_u32 v212, L_fin_%=-L_pc_%=, v212\n"
"v_mfma_f32_16x16x32_bf16 v[180:183], a[188:191], a[196:199], v[180:183]\n"
"s_add_u32 m0, s21, 35840\n"
"ds_read_b64_tr_b16 a[170:171], v207 offset:45408\n"
"v_mfma_f32_16x16x32_bf16 v[176:179], a[188:191], a[192:195], v[176:179]\n"
"s_waitcnt lgkmcnt(4)\n"
"buffer_load_dwordx4 v195, s[36:39], s22 offen lds\n"
"v_mfma_f32_16x16x32_bf16 v[64:67], a[160:163], a[208:211], v[64:67]\n"
"s_mov_b64 s[70:71], s[12:13]\n"
"ds_read_b64_tr_b16 a[172:173], v207 offset:41288\n"
"v_cmp_gt_u32 vcc, 32, v218\n"
"v_mfma_f32_16x16x32_bf16 v[68:71], a[160:163], a[212:215], v[68:71]\n"
"s_nop 0\n"
"v_cndmask_b32 v210, v212, v210, vcc\n"
"v_mov_b32 v212, s70\n"
"v_mfma_f32_16x16x32_bf16 v[72:75], a[160:163], a[216:219], v[72:75]\n"
"ds_read_b64_tr_b16 a[174:175], v207 offset:45416\n"
"v_mov_b32 v213, s76\n"
"v_mfma_f32_16x16x32_bf16 v[76:79], a[160:163], a[220:223], v[76:79]\n"
"s_waitcnt lgkmcnt(4)\n"
"s_add_u32 m0, s21, 36864\n"
"ds_read_b64_tr_b16 a[176:177], v206 offset:41792\n"
"v_cndmask_b32 v220, v213, v212, vcc\n"
"v_mfma_f32_16x16x32_bf16 v[92:95], a[164:167], a[220:223], v[92:95]\n"
"buffer_load_dwordx4 v196, s[36:39], s22 offen lds\n"
"v_mfma_f32_16x16x32_bf16 v[88:91], a[164:167], a[216:219], v[88:91]\n"
"ds_read_b64_tr_b16 a[178:179], v206 offset:45920\n"
"v_mov_b32 v212, s71\n"
"v_mfma_f32_16x16x32_bf16 v[84:87], a[164:167], a[212:215], v[84:87]\n"
"s_add_u32 m0, s21, 37888\n"
"ds_read_b64_tr_b16 a[180:181], v206 offset:41800\n"
"v_mov_b32 v213, s77\n"
"v_mfma_f32_16x16x32_bf16 v[80:83], a[164:167], a[208:211], v[80:83]\n"
"s_waitcnt lgkmcnt(5)\n"
"buffer_load_dwordx4 v197, s[36:39], s22 offen lds\n"
"v_mfma_f32_16x16x32_bf16 v[96:99], a[168:171], a[208:211], v[96:99]\n"
"ds_read_b64_tr_b16 a[182:183], v206 offset:45928\n"
"v_cndmask_b32 v221, v213, v212, vcc\n"
"v_mfma_f32_16x16x32_bf16 v[100:103], a[168:171], a[212:215], v[100:103]\n"
"v_mov_b32 v213, 384\n"
"v_cndmask_b32 v222, 0, v213, vcc\n"
"v_mfma_f32_16x16x32_bf16 v[104:107], a[168:171], a[216:219], v[104:107]\n"
"ds_read_b64_tr_b16 a[184:185], v207 offset:41792\n"
"v_mov_b32 v223, 0\n"
"v_mfma_f32_16x16x32_bf16 v[108:111], a[168:171], a[220:223], v[108:111]\n"
"s_waitcnt lgkmcnt(5)\n"
"s_add_u32 m0, s21, 38912\n"
"ds_read_b64_tr_b16 a[186:187], v207 offset:45920\n"
"v_mov_b32 v211, 0\n"
"v_mfma_f32_16x16x32_bf16 v[124:127], a[172:175], a[220:223], v[124:127]\n"
"buffer_load_dwordx4 v198, s[36:39], s22 offen lds\n"
"v_mfma_f32_16x16x32_bf16 v[120:123], a[172:175], a[216:219], v[120:123]\n"
"ds_read_b64_tr_b16 a[188:189], v207 offset:41800\n"
"v_lshl_add_u64 v[220:221], v[210:211], 0, v[220:221]\n"
"v_mfma_f32_16x16x32_bf16 v[116:119], a[172:175], a[212:215], v[116:119]\n"
"s_add_u32 m0, s21, 39936\n"
"ds_read_b64_tr_b16 a[190:191], v207 offset:45928\n"
"v_mfma_f32_16x16x32_bf16 v[112:115], a[172:175], a[208:211], v[112:115]\n"
"s_waitcnt lgkmcnt(6)\n"
"buffer_load_dwordx4 v199, s[36:39], s22 offen lds\n"
"v_mfma_f32_16x16x32_bf16 v[128:131], a[176:179], a[208:211], v[128:131]\n"
"v_mfma_f32_16x16x32_bf16 v[132:135], a[176:179], a[212:215], v[132:135]\n"
"v_mfma_f32_16x16x32_bf16 v[136:139], a[176:179], a[216:219], v[136:139]\n"
"v_mfma_f32_16x16x32_bf16 v[140:143], a[176:179], a[220:223], v[140:143]\n"
"s_waitcnt lgkmcnt(4)\n"
"v_mfma_f32_16x16x32_bf16 v[156:159], a[180:183], a[220:223], v[156:159]\n"
"s_waitcnt vmcnt(12) lgkmcnt(0)\n"
"s_barrier\n"
"v_mfma_f32_16x16x32_bf16 v[152:155], a[180:183], a[216:219], v[152:155]\n"
"ds_read_b128 a[192:195], v202 offset:0\n"
"v_mfma_f32_16x16x32_bf16 v[148:151], a[180:183], a[212:215], v[148:151]\n"
"ds_read_b64_tr_b16 a[160:161], v206 offset:0\n"
"v_mfma_f32_16x16x32_bf16 v[144:147], a[180:183], a[208:211], v[144:147]\n"
"ds_read_b64_tr_b16 a[162:163], v206 offset:4128\n"
"v_mfma_f32_16x16x32_bf16 v[160:163], a[184:187], a[208:211], v[160:163]\n"
"ds_read_b128 a[196:199], v202 offset:2048\n"
"v_mfma_f32_16x16x32_bf16 v[164:167], a[184:187], a[212:215], v[164:167]\n"
"ds_read_b128 a[200:203], v202 offset:4096\n"
"v_mfma_f32_16x16x32_bf16 v[168:171], a[184:187], a[216:219], v[168:171]\n"
"ds_read_b128 a[204:207], v202 offset:6144\n"
"v_mfma_f32_16x16x32_bf16 v[172:175], a[184:187], a[220:223], v[172:175]\n"
"ds_read_b64_tr_b16 a[164:165], v206 offset:8\n"
"v_mfma_f32_16x16x32_bf16 v[188:191], a[188:191], a[220:223], v[188:191]\n"
"s_mov_b32 s22, 512\n"
"ds_read_b64_tr_b16 a[166:167], v206 offset:4136\n"
"v_mfma_f32_16x16x32_bf16 v[184:187], a[188:191], a[216:219], v[184:187]\n"
"s_mov_b32 s23, 2097152\n"
"ds_read_b64_tr_b16 a[168:169], v207 offset:0\n"
"v_mfma_f32_16x16x32_bf16 v[180:183], a[188:191], a[212:215], v[180:183]\n"
"s_mov_b32 s24, 2359296\n"
"ds_read_b64_tr_b16 a[170:171], v207 offset:4128\n"
"v_mfma_f32_16x16x32_bf16 v[176:179], a[188:191], a[208:211], v[176:179]\n"
"s_waitcnt lgkmcnt(7)\n"
"v_mfma_f32_16x16x32_bf16 v[64:67], a[160:163], a[192:195], v[64:67]\n"
"s_waitcnt lgkmcnt(6)\n"
"ds_read_b64_tr_b16 a[172:173], v207 offset:8\n"
"v_mfma_f32_16x16x32_bf16 v[68:71], a[160:163], a[196:199], v[68:71]\n"
"s_waitcnt lgkmcnt(6)\n"
"v_mfma_f32_16x16x32_bf16 v[72:75], a[160:163], a[200:203], v[72:75]\n"
"s_waitcnt lgkmcnt(5)\n"
"ds_read_b64_tr_b16 a[174:175], v207 offset:4136\n"
"v_mfma_f32_16x16x32_bf16 v[76:79], a[160:163], a[204:207], v[76:79]\n"
"s_waitcnt lgkmcnt(4)\n"
"s_add_u32 m0, s31, 98560\n"
"ds_read_b64_tr_b16 a[176:177], v206 offset:512\n"
"v_mfma_f32_16x16x32_bf16 v[92:95], a[164:167], a[204:207], v[92:95]\n"
"buffer_load_dwordx4 v200, s[40:43], s23 offen lds\n"
"v_mfma_f32_16x16x32_bf16 v[88:91], a[164:167], a[200:203], v[88:91]\n"
"ds_read_b64_tr_b16 a[178:179], v206 offset:4640\n"
"v_mfma_f32_16x16x32_bf16 v[84:87], a[164:167], a[196:199], v[84:87]\n"
"s_add_u32 m0, s31, 102688\n"
"ds_read_b128 a[208:211], v203 offset:0\n"
"v_mfma_f32_16x16x32_bf16 v[80:83], a[164:167], a[192:195], v[80:83]\n"
"s_waitcnt lgkmcnt(5)\n"
"buffer_load_dwordx4 v201, s[40:43], s23 offen lds\n"
"v_mfma_f32_16x16x32_bf16 v[96:99], a[168:171], a[192:195], v[96:99]\n"
"ds_read_b64_tr_b16 a[180:181], v206 offset:520\n"
"v_mfma_f32_16x16x32_bf16 v[100:103], a[168:171], a[196:199], v[100:103]\n"
"v_mfma_f32_16x16x32_bf16 v[104:107], a[168:171], a[200:203], v[104:107]\n"
"ds_read_b64_tr_b16 a[182:183], v206 offset:4648\n"
"v_mfma_f32_16x16x32_bf16 v[108:111], a[168:171], a[204:207], v[108:111]\n"
"s_waitcnt lgkmcnt(5)\n"
"s_add_u32 m0, s31, 106816\n"
"ds_read_b128 a[212:215], v203 offset:2048\n"
"v_mfma_f32_16x16x32_bf16 v[124:127], a[172:175], a[204:207], v[124:127]\n"
"buffer_load_dwordx4 v200, s[40:43], s24 offen lds\n"
"v_mfma_f32_16x16x32_bf16 v[120:123], a[172:175], a[200:203], v[120:123]\n"
"ds_read_b64_tr_b16 a[184:185], v207 offset:512\n"
"v_mfma_f32_16x16x32_bf16 v[116:119], a[172:175], a[196:199], v[116:119]\n"
"s_add_u32 m0, s31, 110944\n"
"ds_read_b64_tr_b16 a[186:187], v207 offset:4640\n"
"v_mfma_f32_16x16x32_bf16 v[112:115], a[172:175], a[192:195], v[112:115]\n"
"s_waitcnt lgkmcnt(6)\n"
"buffer_load_dwordx4 v201, s[40:43], s24 offen lds\n"
"v_mfma_f32_16x16x32_bf16 v[128:131], a[176:179], a[192:195], v[128:131]\n"
"ds_read_b128 a[216:219], v203 offset:4096\n"
"v_mfma_f32_16x16x32_bf16 v[132:135], a[176:179], a[196:199], v[132:135]\n"
"v_mfma_f32_16x16x32_bf16 v[136:139], a[176:179], a[200:203], v[136:139]\n"
"ds_read_b64_tr_b16 a[188:189], v207 offset:520\n"
"v_mfma_f32_16x16x32_bf16 v[140:143], a[176:179], a[204:207], v[140:143]\n"
"s_waitcnt lgkmcnt(5)\n"
"s_add_u32 m0, s21, 131072\n"
"ds_read_b64_tr_b16 a[190:191], v207 offset:4648\n"
"v_mfma_f32_16x16x32_bf16 v[156:159], a[180:183], a[204:207], v[156:159]\n"
"buffer_load_dwordx4 v192, s[36:39], s22 offen lds\n"
"v_mfma_f32_16x16x32_bf16 v[152:155], a[180:183], a[200:203], v[152:155]\n"
"ds_read_b128 a[220:223], v203 offset:6144\n"
"v_mfma_f32_16x16x32_bf16 v[148:151], a[180:183], a[196:199], v[148:151]\n"
"s_add_u32 m0, s21, 132096\n"
"ds_read_b64_tr_b16 a[160:161], v206 offset:8256\n"
"v_mfma_f32_16x16x32_bf16 v[144:147], a[180:183], a[192:195], v[144:147]\n"
"s_waitcnt lgkmcnt(5)\n"
"buffer_load_dwordx4 v193, s[36:39], s22 offen lds\n"
"v_mfma_f32_16x16x32_bf16 v[160:163], a[184:187], a[192:195], v[160:163]\n"
"ds_read_b64_tr_b16 a[162:163], v206 offset:12384\n"
"v_mfma_f32_16x16x32_bf16 v[164:167], a[184:187], a[196:199], v[164:167]\n"
"v_mfma_f32_16x16x32_bf16 v[168:171], a[184:187], a[200:203], v[168:171]\n"
"ds_read_b64_tr_b16 a[164:165], v206 offset:8264\n"
"v_mfma_f32_16x16x32_bf16 v[172:175], a[184:187], a[204:207], v[172:175]\n"
"s_waitcnt lgkmcnt(4)\n"
"s_add_u32 m0, s21, 133120\n"
"ds_read_b64_tr_b16 a[166:167], v206 offset:12392\n"
"v_mfma_f32_16x16x32_bf16 v[188:191], a[188:191], a[204:207], v[188:191]\n"
"buffer_load_dwordx4 v194, s[36:39], s22 offen lds\n"
"v_mfma_f32_16x16x32_bf16 v[184:187], a[188:191], a[200:203], v[184:187]\n"
"ds_read_b64_tr_b16 a[168:169], v207 offset:8256\n"
"v_mfma_f32_16x16x32_bf16 v[180:183], a[188:191], a[196:199], v[180:183]\n"
"s_add_u32 m0, s21, 134144\n"
"ds_read_b64_tr_b16 a[170:171], v207 offset:12384\n"
"v_mfma_f32_16x16x32_bf16 v[176:179], a[188:191], a[192:195], v[176:179]\n"
"s_waitcnt lgkmcnt(4)\n"
"buffer_load_dwordx4 v195, s[36:39], s22 offen lds\n"
"v_mfma_f32_16x16x32_bf16 v[64:67], a[160:163], a[208:211], v[64:67]\n"
"ds_read_b64_tr_b16 a[172:173], v207 offset:8264\n"
"v_mfma_f32_16x16x32_bf16 v[68:71], a[160:163], a[212:215], v[68:71]\n"
"v_mfma_f32_16x16x32_bf16 v[72:75], a[160:163], a[216:219], v[72:75]\n"
"ds_read_b64_tr_b16 a[174:175], v207 offset:12392\n"
"v_mfma_f32_16x16x32_bf16 v[76:79], a[160:163], a[220:223], v[76:79]\n"
"s_waitcnt lgkmcnt(4)\n"
"s_add_u32 m0, s21, 135168\n"
"ds_read_b64_tr_b16 a[176:177], v206 offset:8768\n"
"v_mfma_f32_16x16x32_bf16 v[92:95], a[164:167], a[220:223], v[92:95]\n"
"buffer_load_dwordx4 v196, s[36:39], s22 offen lds\n"
"v_mfma_f32_16x16x32_bf16 v[88:91], a[164:167], a[216:219], v[88:91]\n"
"ds_read_b64_tr_b16 a[178:179], v206 offset:12896\n"
"v_mfma_f32_16x16x32_bf16 v[84:87], a[164:167], a[212:215], v[84:87]\n"
"s_add_u32 m0, s21, 136192\n"
"ds_read_b64_tr_b16 a[180:181], v206 offset:8776\n"
"v_mfma_f32_16x16x32_bf16 v[80:83], a[164:167], a[208:211], v[80:83]\n"
"s_waitcnt lgkmcnt(5)\n"
"buffer_load_dwordx4 v197, s[36:39], s22 offen lds\n"
"v_mfma_f32_16x16x32_bf16 v[96:99], a[168:171], a[208:211], v[96:99]\n"
"ds_read_b64_tr_b16 a[182:183], v206 offset:12904\n"
"v_mfma_f32_16x16x32_bf16 v[100:103], a[168:171], a[212:215], v[100:103]\n"
"v_mfma_f32_16x16x32_bf16 v[104:107], a[168:171], a[216:219], v[104:107]\n"
"ds_read_b64_tr_b16 a[184:185], v207 offset:8768\n"
"v_mfma_f32_16x16x32_bf16 v[108:111], a[168:171], a[220:223], v[108:111]\n"
"s_waitcnt lgkmcnt(5)\n"
"s_add_u32 m0, s21, 137216\n"
"ds_read_b64_tr_b16 a[186:187], v207 offset:12896\n"
"v_mfma_f32_16x16x32_bf16 v[124:127], a[172:175], a[220:223], v[124:127]\n"
"buffer_load_dwordx4 v198, s[36:39], s22 offen lds\n"
"v_mfma_f32_16x16x32_bf16 v[120:123], a[172:175], a[216:219], v[120:123]\n"
"ds_read_b64_tr_b16 a[188:189], v207 offset:8776\n"
"v_mfma_f32_16x16x32_bf16 v[116:119], a[172:175], a[212:215], v[116:119]\n"
"s_add_u32 m0, s21, 138240\n"
"ds_read_b64_tr_b16 a[190:191], v207 offset:12904\n"
"v_mfma_f32_16x16x32_bf16 v[112:115], a[172:175], a[208:211], v[112:115]\n"
"s_waitcnt lgkmcnt(6)\n"
"buffer_load_dwordx4 v199, s[36:39], s22 offen lds\n"
"v_mfma_f32_16x16x32_bf16 v[128:131], a[176:179], a[208:211], v[128:131]\n"
"v_mfma_f32_16x16x32_bf16 v[132:135], a[176:179], a[212:215], v[132:135]\n"
"v_mfma_f32_16x16x32_bf16 v[136:139], a[176:179], a[216:219], v[136:139]\n"
"v_mfma_f32_16x16x32_bf16 v[140:143], a[176:179], a[220:223], v[140:143]\n"
"s_waitcnt lgkmcnt(4)\n"
"v_mfma_f32_16x16x32_bf16 v[156:159], a[180:183], a[220:223], v[156:159]\n"
"s_waitcnt vmcnt(12) lgkmcnt(0)\n"
"s_barrier\n"
"v_mfma_f32_16x16x32_bf16 v[152:155], a[180:183], a[216:219], v[152:155]\n"
"ds_read_b128 a[192:195], v202 offset:32768\n"
"v_mfma_f32_16x16x32_bf16 v[148:151], a[180:183], a[212:215], v[148:151]\n"
"ds_read_b64_tr_b16 a[160:161], v206 offset:16512\n"
"v_mfma_f32_16x16x32_bf16 v[144:147], a[180:183], a[208:211], v[144:147]\n"
"ds_read_b64_tr_b16 a[162:163], v206 offset:20640\n"
"v_mfma_f32_16x16x32_bf16 v[160:163], a[184:187], a[208:211], v[160:163]\n"
"ds_read_b128 a[196:199], v202 offset:34816\n"
"v_mfma_f32_16x16x32_bf16 v[164:167], a[184:187], a[212:215], v[164:167]\n"
"ds_read_b128 a[200:203], v202 offset:36864\n"
"v_mfma_f32_16x16x32_bf16 v[168:171], a[184:187], a[216:219], v[168:171]\n"
"ds_read_b128 a[204:207], v202 offset:38912\n"
"v_mfma_f32_16x16x32_bf16 v[172:175], a[184:187], a[220:223], v[172:175]\n"
"ds_read_b64_tr_b16 a[164:165], v206 offset:16520\n"
"v_mfma_f32_16x16x32_bf16 v[188:191], a[188:191], a[220:223], v[188:191]\n"
"s_mov_b32 s22, 640\n"
"ds_read_b64_tr_b16 a[166:167], v206 offset:20648\n"
"v_mfma_f32_16x16x32_bf16 v[184:187], a[188:191], a[216:219], v[184:187]\n"
"s_mov_b32 s23, 2621440\n"
"ds_read_b64_tr_b16 a[168:169], v207 offset:16512\n"
"v_mfma_f32_16x16x32_bf16 v[180:183], a[188:191], a[212:215], v[180:183]\n"
"s_mov_b32 s24, 2883584\n"
"ds_read_b64_tr_b16 a[170:171], v207 offset:20640\n"
"v_mfma_f32_16x16x32_bf16 v[176:179], a[188:191], a[208:211], v[176:179]\n"
"s_waitcnt lgkmcnt(7)\n"
"v_mfma_f32_16x16x32_bf16 v[64:67], a[160:163], a[192:195], v[64:67]\n"
"s_waitcnt lgkmcnt(6)\n"
"ds_read_b64_tr_b16 a[172:173], v207 offset:16520\n"
"v_mfma_f32_16x16x32_bf16 v[68:71], a[160:163], a[196:199], v[68:71]\n"
"s_waitcnt lgkmcnt(6)\n"
"v_mfma_f32_16x16x32_bf16 v[72:75], a[160:163], a[200:203], v[72:75]\n"
"s_waitcnt lgkmcnt(5)\n"
"ds_read_b64_tr_b16 a[174:175], v207 offset:20648\n"
"v_mfma_f32_16x16x32_bf16 v[76:79], a[160:163], a[204:207], v[76:79]\n"
"s_waitcnt lgkmcnt(4)\n"
"s_add_u32 m0, s31, 65536\n"
"ds_read_b64_tr_b16 a[176:177], v206 offset:17024\n"
"v_mfma_f32_16x16x32_bf16 v[92:95], a[164:167], a[204:207], v[92:95]\n"
"buffer_load_dwordx4 v200, s[40:43], s23 offen lds\n"
"v_mfma_f32_16x16x32_bf16 v[88:91], a[164:167], a[200:203], v[88:91]\n"
"ds_read_b64_tr_b16 a[178:179], v206 offset:21152\n"
"v_mfma_f32_16x16x32_bf16 v[84:87], a[164:167], a[196:199], v[84:87]\n"
"s_add_u32 m0, s31, 69664\n"
"ds_read_b128 a[208:211], v203 offset:32768\n"
"v_mfma_f32_16x16x32_bf16 v[80:83], a[164:167], a[192:195], v[80:83]\n"
"s_waitcnt lgkmcnt(5)\n"
"buffer_load_dwordx4 v201, s[40:43], s23 offen lds\n"
"v_mfma_f32_16x16x32_bf16 v[96:99], a[168:171], a[192:195], v[96:99]\n"
"ds_read_b64_tr_b16 a[180:181], v206 offset:17032\n"
"v_mfma_f32_16x16x32_bf16 v[100:103], a[168:171], a[196:199], v[100:103]\n"
"v_mfma_f32_16x16x32_bf16 v[104:107], a[168:171], a[200:203], v[104:107]\n"
"ds_read_b64_tr_b16 a[182:183], v206 offset:21160\n"
"v_mfma_f32_16x16x32_bf16 v[108:111], a[168:171], a[204:207], v[108:111]\n"
"s_waitcnt lgkmcnt(5)\n"
"s_add_u32 m0, s31, 73792\n"
"ds_read_b128 a[212:215], v203 offset:34816\n"
"v_mfma_f32_16x16x32_bf16 v[124:127], a[172:175], a[204:207], v[124:127]\n"
"buffer_load_dwordx4 v200, s[40:43], s24 offen lds\n"
"v_mfma_f32_16x16x32_bf16 v[120:123], a[172:175], a[200:203], v[120:123]\n"
"ds_read_b64_tr_b16 a[184:185], v207 offset:17024\n"
"v_mfma_f32_16x16x32_bf16 v[116:119], a[172:175], a[196:199], v[116:119]\n"
"s_add_u32 m0, s31, 77920\n"
"ds_read_b64_tr_b16 a[186:187], v207 offset:21152\n"
"v_mfma_f32_16x16x32_bf16 v[112:115], a[172:175], a[192:195], v[112:115]\n"
"s_waitcnt lgkmcnt(6)\n"
"buffer_load_dwordx4 v201, s[40:43], s24 offen lds\n"
"v_mfma_f32_16x16x32_bf16 v[128:131], a[176:179], a[192:195], v[128:131]\n"
"ds_read_b128 a[216:219], v203 offset:36864\n"
"v_mfma_f32_16x16x32_bf16 v[132:135], a[176:179], a[196:199], v[132:135]\n"
"v_mfma_f32_16x16x32_bf16 v[136:139], a[176:179], a[200:203], v[136:139]\n"
"ds_read_b64_tr_b16 a[188:189], v207 offset:17032\n"
"v_mfma_f32_16x16x32_bf16 v[140:143], a[176:179], a[204:207], v[140:143]\n"
"s_waitcnt lgkmcnt(5)\n"
"s_add_u32 m0, s21, 0\n"
"ds_read_b64_tr_b16 a[190:191], v207 offset:21160\n"
"v_mfma_f32_16x16x32_bf16 v[156:159], a[180:183], a[204:207], v[156:159]\n"
"buffer_load_dwordx4 v192, s[36:39], s22 offen lds\n"
"v_mfma_f32_16x16x32_bf16 v[152:155], a[180:183], a[200:203], v[152:155]\n"
"ds_read_b128 a[220:223], v203 offset:38912\n"
"v_mfma_f32_16x16x32_bf16 v[148:151], a[180:183], a[196:199], v[148:151]\n"
"s_add_u32 m0, s21, 1024\n"
"ds_read_b64_tr_b16 a[160:161], v206 offset:24768\n"
"v_mfma_f32_16x16x32_bf16 v[144:147], a[180:183], a[192:195], v[144:147]\n"
"s_waitcnt lgkmcnt(5)\n"
"buffer_load_dwordx4 v193, s[36:39], s22 offen lds\n"
"v_mfma_f32_16x16x32_bf16 v[160:163], a[184:187], a[192:195], v[160:163]\n"
"ds_read_b64_tr_b16 a[162:163], v206 offset:28896\n"
"v_mfma_f32_16x16x32_bf16 v[164:167], a[184:187], a[196:199], v[164:167]\n"
"v_mfma_f32_16x16x32_bf16 v[168:171], a[184:187], a[200:203], v[168:171]\n"
"ds_read_b64_tr_b16 a[164:165], v206 offset:24776\n"
"v_mfma_f32_16x16x32_bf16 v[172:175], a[184:187], a[204:207], v[172:175]\n"
"s_waitcnt lgkmcnt(4)\n"
"s_add_u32 m0, s21, 2048\n"
"ds_read_b64_tr_b16 a[166:167], v206 offset:28904\n"
"v_mfma_f32_16x16x32_bf16 v[188:191], a[188:191], a[204:207], v[188:191]\n"
"buffer_load_dwordx4 v194, s[36:39], s22 offen lds\n"
"v_mfma_f32_16x16x32_bf16 v[184:187], a[188:191], a[200:203], v[184:187]\n"
"ds_read_b64_tr_b16 a[168:169], v207 offset:24768\n"
"v_mfma_f32_16x16x32_bf16 v[180:183], a[188:191], a[196:199], v[180:183]\n"
"s_add_u32 m0, s21, 3072\n"
"ds_read_b64_tr_b16 a[170:171], v207 offset:28896\n"
"v_mfma_f32_16x16x32_bf16 v[176:179], a[188:191], a[192:195], v[176:179]\n"
"s_waitcnt lgkmcnt(4)\n"
"buffer_load_dwordx4 v195, s[36:39], s22 offen lds\n"
"v_mfma_f32_16x16x32_bf16 v[64:67], a[160:163], a[208:211], v[64:67]\n"
"ds_read_b64_tr_b16 a[172:173], v207 offset:24776\n"
"v_mfma_f32_16x16x32_bf16 v[68:71], a[160:163], a[212:215], v[68:71]\n"
"v_mfma_f32_16x16x32_bf16 v[72:75], a[160:163], a[216:219], v[72:75]\n"
"ds_read_b64_tr_b16 a[174:175], v207 offset:28904\n"
"v_mfma_f32_16x16x32_bf16 v[76:79], a[160:163], a[220:223], v[76:79]\n"
"s_waitcnt lgkmcnt(4)\n"
"s_add_u32 m0, s21, 4096\n"
"ds_read_b64_tr_b16 a[176:177], v206 offset:25280\n"
"v_mfma_f32_16x16x32_bf16 v[92:95], a[164:167], a[220:223], v[92:95]\n"
"buffer_load_dwordx4 v196, s[36:39], s22 offen lds\n"
"v_mfma_f32_16x16x32_bf16 v[88:91], a[164:167], a[216:219], v[88:91]\n"
"ds_read_b64_tr_b16 a[178:179], v206 offset:29408\n"
"v_mfma_f32_16x16x32_bf16 v[84:87], a[164:167], a[212:215], v[84:87]\n"
"s_add_u32 m0, s21, 5120\n"
"ds_read_b64_tr_b16 a[180:181], v206 offset:25288\n"
"v_mfma_f32_16x16x32_bf16 v[80:83], a[164:167], a[208:211], v[80:83]\n"
"s_waitcnt lgkmcnt(5)\n"
"buffer_load_dwordx4 v197, s[36:39], s22 offen lds\n"
"v_mfma_f32_16x16x32_bf16 v[96:99], a[168:171], a[208:211], v[96:99]\n"
"ds_read_b64_tr_b16 a[182:183], v206 offset:29416\n"
"v_mfma_f32_16x16x32_bf16 v[100:103], a[168:171], a[212:215], v[100:103]\n"
"v_mfma_f32_16x16x32_bf16 v[104:107], a[168:171], a[216:219], v[104:107]\n"
"ds_read_b64_tr_b16 a[184:185], v207 offset:25280\n"
"v_mfma_f32_16x16x32_bf16 v[108:111], a[168:171], a[220:223], v[108:111]\n"
"s_waitcnt lgkmcnt(5)\n"
"s_add_u32 m0, s21, 6144\n"
"ds_read_b64_tr_b16 a[186:187], v207 offset:29408\n"
"v_mfma_f32_16x16x32_bf16 v[124:127], a[172:175], a[220:223], v[124:127]\n"
"buffer_load_dwordx4 v198, s[36:39], s22 offen lds\n"
"v_mfma_f32_16x16x32_bf16 v[120:123], a[172:175], a[216:219], v[120:123]\n"
"ds_read_b64_tr_b16 a[188:189], v207 offset:25288\n"
"v_mfma_f32_16x16x32_bf16 v[116:119], a[172:175], a[212:215], v[116:119]\n"
"s_add_u32 m0, s21, 7168\n"
"ds_read_b64_tr_b16 a[190:191], v207 offset:29416\n"
"v_mfma_f32_16x16x32_bf16 v[112:115], a[172:175], a[208:211], v[112:115]\n"
"s_waitcnt lgkmcnt(6)\n"
"buffer_load_dwordx4 v199, s[36:39], s22 offen lds\n"
"v_mfma_f32_16x16x32_bf16 v[128:131], a[176:179], a[208:211], v[128:131]\n"
"v_mfma_f32_16x16x32_bf16 v[132:135], a[176:179], a[212:215], v[132:135]\n"
"v_mfma_f32_16x16x32_bf16 v[136:139], a[176:179], a[216:219], v[136:139]\n"
"v_mfma_f32_16x16x32_bf16 v[140:143], a[176:179], a[220:223], v[140:143]\n"
"s_waitcnt lgkmcnt(4)\n"
"v_mfma_f32_16x16x32_bf16 v[156:159], a[180:183], a[220:223], v[156:159]\n"
"s_waitcnt vmcnt(12) lgkmcnt(0)\n"
"s_barrier\n"
"v_mfma_f32_16x16x32_bf16 v[152:155], a[180:183], a[216:219], v[152:155]\n"
"ds_read_b128 a[192:195], v204 offset:0\n"
"v_mfma_f32_16x16x32_bf16 v[148:151], a[180:183], a[212:215], v[148:151]\n"
"ds_read_b64_tr_b16 a[160:161], v206 offset:33024\n"
"v_mfma_f32_16x16x32_bf16 v[144:147], a[180:183], a[208:211], v[144:147]\n"
"ds_read_b64_tr_b16 a[162:163], v206 offset:37152\n"
"v_mfma_f32_16x16x32_bf16 v[160:163], a[184:187], a[208:211], v[160:163]\n"
"ds_read_b128 a[196:199], v204 offset:2048\n"
"v_mfma_f32_16x16x32_bf16 v[164:167], a[184:187], a[212:215], v[164:167]\n"
"ds_read_b128 a[200:203], v204 offset:4096\n"
"v_mfma_f32_16x16x32_bf16 v[168:171], a[184:187], a[216:219], v[168:171]\n"
"ds_read_b128 a[204:207], v204 offset:6144\n"
"v_mfma_f32_16x16x32_bf16 v[172:175], a[184:187], a[220:223], v[172:175]\n"
"ds_read_b64_tr_b16 a[164:165], v206 offset:33032\n"
"v_mfma_f32_16x16x32_bf16 v[188:191], a[188:191], a[220:223], v[188:191]\n"
"s_mov_b32 s22, 768\n"
"ds_read_b64_tr_b16 a[166:167], v206 offset:37160\n"
"v_mfma_f32_16x16x32_bf16 v[184:187], a[188:191], a[216:219], v[184:187]\n"
"s_mov_b32 s23, 3145728\n"
"ds_read_b64_tr_b16 a[168:169], v207 offset:33024\n"
"v_mfma_f32_16x16x32_bf16 v[180:183], a[188:191], a[212:215], v[180:183]\n"
"s_mov_b32 s24, 3407872\n"
"ds_read_b64_tr_b16 a[170:171], v207 offset:37152\n"
"v_mfma_f32_16x16x32_bf16 v[176:179], a[188:191], a[208:211], v[176:179]\n"
"s_waitcnt lgkmcnt(7)\n"
"v_mfma_f32_16x16x32_bf16 v[64:67], a[160:163], a[192:195], v[64:67]\n"
"s_waitcnt lgkmcnt(6)\n"
"ds_read_b64_tr_b16 a[172:173], v207 offset:33032\n"
"v_mfma_f32_16x16x32_bf16 v[68:71], a[160:163], a[196:199], v[68:71]\n"
"s_waitcnt lgkmcnt(6)\n"
"v_mfma_f32_16x16x32_bf16 v[72:75], a[160:163], a[200:203], v[72:75]\n"
"s_waitcnt lgkmcnt(5)\n"
"ds_read_b64_tr_b16 a[174:175], v207 offset:37160\n"
"v_mfma_f32_16x16x32_bf16 v[76:79], a[160:163], a[204:207], v[76:79]\n"
"s_waitcnt lgkmcnt(4)\n"
"s_add_u32 m0, s31, 82048\n"
"ds_read_b64_tr_b16 a[176:177], v206 offset:33536\n"
"v_mfma_f32_16x16x32_bf16 v[92:95], a[164:167], a[204:207], v[92:95]\n"
"buffer_load_dwordx4 v200, s[40:43], s23 offen lds\n"
"v_mfma_f32_16x16x32_bf16 v[88:91], a[164:167], a[200:203], v[88:91]\n"
"ds_read_b64_tr_b16 a[178:179], v206 offset:37664\n"
"v_mfma_f32_16x16x32_bf16 v[84:87], a[164:167], a[196:199], v[84:87]\n"
"s_add_u32 m0, s31, 86176\n"
"ds_read_b128 a[208:211], v205 offset:0\n"
"v_mfma_f32_16x16x32_bf16 v[80:83], a[164:167], a[192:195], v[80:83]\n"
"s_waitcnt lgkmcnt(5)\n"
"buffer_load_dwordx4 v201, s[40:43], s23 offen lds\n"
"v_mfma_f32_16x16x32_bf16 v[96:99], a[168:171], a[192:195], v[96:99]\n"
"ds_read_b64_tr_b16 a[180:181], v206 offset:33544\n"
"v_mfma_f32_16x16x32_bf16 v[100:103], a[168:171], a[196:199], v[100:103]\n"
"v_mfma_f32_16x16x32_bf16 v[104:107], a[168:171], a[200:203], v[104:107]\n"
"ds_read_b64_tr_b16 a[182:183], v206 offset:37672\n"
"v_mfma_f32_16x16x32_bf16 v[108:111], a[168:171], a[204:207], v[108:111]\n"
"s_waitcnt lgkmcnt(5)\n"
"s_add_u32 m0, s31, 90304\n"
"ds_read_b128 a[212:215], v205 offset:2048\n"
"v_mfma_f32_16x16x32_bf16 v[124:127], a[172:175], a[204:207], v[124:127]\n"
"buffer_load_dwordx4 v200, s[40:43], s24 offen lds\n"
"v_mfma_f32_16x16x32_bf16 v[120:123], a[172:175], a[200:203], v[120:123]\n"
"ds_read_b64_tr_b16 a[184:185], v207 offset:33536\n"
"v_mfma_f32_16x16x32_bf16 v[116:119], a[172:175], a[196:199], v[116:119]\n"
"s_add_u32 m0, s31, 94432\n"
"ds_read_b64_tr_b16 a[186:187], v207 offset:37664\n"
"v_mfma_f32_16x16x32_bf16 v[112:115], a[172:175], a[192:195], v[112:115]\n"
"s_waitcnt lgkmcnt(6)\n"
"buffer_load_dwordx4 v201, s[40:43], s24 offen lds\n"
"v_mfma_f32_16x16x32_bf16 v[128:131], a[176:179], a[192:195], v[128:131]\n"
"ds_read_b128 a[216:219], v205 offset:4096\n"
"v_mfma_f32_16x16x32_bf16 v[132:135], a[176:179], a[196:199], v[132:135]\n"
"v_mfma_f32_16x16x32_bf16 v[136:139], a[176:179], a[200:203], v[136:139]\n"
"ds_read_b64_tr_b16 a[188:189], v207 offset:33544\n"
"v_mfma_f32_16x16x32_bf16 v[140:143], a[176:179], a[204:207], v[140:143]\n"
"s_waitcnt lgkmcnt(5)\n"
"s_add_u32 m0, s21, 32768\n"
"ds_read_b64_tr_b16 a[190:191], v207 offset:37672\n"
"v_mfma_f32_16x16x32_bf16 v[156:159], a[180:183], a[204:207], v[156:159]\n"
"buffer_load_dwordx4 v192, s[36:39], s22 offen lds\n"
"v_mfma_f32_16x16x32_bf16 v[152:155], a[180:183], a[200:203], v[152:155]\n"
"ds_read_b128 a[220:223], v205 offset:6144\n"
"v_mfma_f32_16x16x32_bf16 v[148:151], a[180:183], a[196:199], v[148:151]\n"
"s_add_u32 m0, s21, 33792\n"
"ds_read_b64_tr_b16 a[160:161], v206 offset:41280\n"
"v_mfma_f32_16x16x32_bf16 v[144:147], a[180:183], a[192:195], v[144:147]\n"
"s_waitcnt lgkmcnt(5)\n"
"buffer_load_dwordx4 v193, s[36:39], s22 offen lds\n"
"v_mfma_f32_16x16x32_bf16 v[160:163], a[184:187], a[192:195], v[160:163]\n"
"ds_read_b64_tr_b16 a[162:163], v206 offset:45408\n"
"v_mfma_f32_16x16x32_bf16 v[164:167], a[184:187], a[196:199], v[164:167]\n"
"v_mfma_f32_16x16x32_bf16 v[168:171], a[184:187], a[200:203], v[168:171]\n"
"ds_read_b64_tr_b16 a[164:165], v206 offset:41288\n"
"v_mfma_f32_16x16x32_bf16 v[172:175], a[184:187], a[204:207], v[172:175]\n"
"s_waitcnt lgkmcnt(4)\n"
"s_add_u32 m0, s21, 34816\n"
"ds_read_b64_tr_b16 a[166:167], v206 offset:45416\n"
"v_mfma_f32_16x16x32_bf16 v[188:191], a[188:191], a[204:207], v[188:191]\n"
"buffer_load_dwordx4 v194, s[36:39], s22 offen lds\n"
"v_mfma_f32_16x16x32_bf16 v[184:187], a[188:191], a[200:203], v[184:187]\n"
"ds_read_b64_tr_b16 a[168:169], v207 offset:41280\n"
"v_mfma_f32_16x16x32_bf16 v[180:183], a[188:191], a[196:199], v[180:183]\n"
"s_add_u32 m0, s21, 35840\n"
"ds_read_b64_tr_b16 a[170:171], v207 offset:45408\n"
"v_mfma_f32_16x16x32_bf16 v[176:179], a[188:191], a[192:195], v[176:179]\n"
"s_waitcnt lgkmcnt(4)\n"
"buffer_load_dwordx4 v195, s[36:39], s22 offen lds\n"
"v_mfma_f32_16x16x32_bf16 v[64:67], a[160:163], a[208:211], v[64:67]\n"
"ds_read_b64_tr_b16 a[172:173], v207 offset:41288\n"
"v_mfma_f32_16x16x32_bf16 v[68:71], a[160:163], a[212:215], v[68:71]\n"
"v_mfma_f32_16x16x32_bf16 v[72:75], a[160:163], a[216:219], v[72:75]\n"
"ds_read_b64_tr_b16 a[174:175], v207 offset:45416\n"
"v_mfma_f32_16x16x32_bf16 v[76:79], a[160:163], a[220:223], v[76:79]\n"
"s_waitcnt lgkmcnt(4)\n"
"s_add_u32 m0, s21, 36864\n"
"ds_read_b64_tr_b16 a[176:177], v206 offset:41792\n"
"v_mfma_f32_16x16x32_bf16 v[92:95], a[164:167], a[220:223], v[92:95]\n"
"buffer_load_dwordx4 v196, s[36:39], s22 offen lds\n"
"v_mfma_f32_16x16x32_bf16 v[88:91], a[164:167], a[216:219], v[88:91]\n"
"ds_read_b64_tr_b16 a[178:179], v206 offset:45920\n"
"v_mfma_f32_16x16x32_bf16 v[84:87], a[164:167], a[212:215], v[84:87]\n"
"s_add_u32 m0, s21, 37888\n"
"ds_read_b64_tr_b16 a[180:181], v206 offset:41800\n"
"v_mfma_f32_16x16x32_bf16 v[80:83], a[164:167], a[208:211], v[80:83]\n"
"s_waitcnt lgkmcnt(5)\n"
"buffer_load_dwordx4 v197, s[36:39], s22 offen lds\n"
"v_mfma_f32_16x16x32_bf16 v[96:99], a[168:171], a[208:211], v[96:99]\n"
"ds_read_b64_tr_b16 a[182:183], v206 offset:45928\n"
"v_mfma_f32_16x16x32_bf16 v[100:103], a[168:171], a[212:215], v[100:103]\n"
"v_mfma_f32_16x16x32_bf16 v[104:107], a[168:171], a[216:219], v[104:107]\n"
"ds_read_b64_tr_b16 a[184:185], v207 offset:41792\n"
"v_mfma_f32_16x16x32_bf16 v[108:111], a[168:171], a[220:223], v[108:111]\n"
"s_waitcnt lgkmcnt(5)\n"
"s_add_u32 m0, s21, 38912\n"
"ds_read_b64_tr_b16 a[186:187], v207 offset:45920\n"
"v_mfma_f32_16x16x32_bf16 v[124:127], a[172:175], a[220:223], v[124:127]\n"
"buffer_load_dwordx4 v198, s[36:39], s22 offen lds\n"
"v_mfma_f32_16x16x32_bf16 v[120:123], a[172:175], a[216:219], v[120:123]\n"
"ds_read_b64_tr_b16 a[188:189], v207 offset:41800\n"
"v_mfma_f32_16x16x32_bf16 v[116:119], a[172:175], a[212:215], v[116:119]\n"
"s_add_u32 m0, s21, 39936\n"
"ds_read_b64_tr_b16 a[190:191], v207 offset:45928\n"
"v_mfma_f32_16x16x32_bf16 v[112:115], a[172:175], a[208:211], v[112:115]\n"
"s_waitcnt lgkmcnt(6)\n"
"buffer_load_dwordx4 v199, s[36:39], s22 offen lds\n"
"v_mfma_f32_16x16x32_bf16 v[128:131], a[176:179], a[208:211], v[128:131]\n"
"v_mfma_f32_16x16x32_bf16 v[132:135], a[176:179], a[212:215], v[132:135]\n"
"v_mfma_f32_16x16x32_bf16 v[136:139], a[176:179], a[216:219], v[136:139]\n"
"v_mfma_f32_16x16x32_bf16 v[140:143], a[176:179], a[220:223], v[140:143]\n"
"s_waitcnt lgkmcnt(4)\n"
"v_mfma_f32_16x16x32_bf16 v[156:159], a[180:183], a[220:223], v[156:159]\n"
"s_waitcnt vmcnt(12) lgkmcnt(0)\n"
"s_barrier\n"
"v_mfma_f32_16x16x32_bf16 v[152:155], a[180:183], a[216:219], v[152:155]\n"
"ds_read_b128 a[192:195], v202 offset:0\n"
"v_mfma_f32_16x16x32_bf16 v[148:151], a[180:183], a[212:215], v[148:151]\n"
"ds_read_b64_tr_b16 a[160:161], v206 offset:0\n"
"v_mfma_f32_16x16x32_bf16 v[144:147], a[180:183], a[208:211], v[144:147]\n"
"ds_read_b64_tr_b16 a[162:163], v206 offset:4128\n"
"v_mfma_f32_16x16x32_bf16 v[160:163], a[184:187], a[208:211], v[160:163]\n"
"ds_read_b128 a[196:199], v202 offset:2048\n"
"v_mfma_f32_16x16x32_bf16 v[164:167], a[184:187], a[212:215], v[164:167]\n"
"ds_read_b128 a[200:203], v202 offset:4096\n"
"v_mfma_f32_16x16x32_bf16 v[168:171], a[184:187], a[216:219], v[168:171]\n"
"ds_read_b128 a[204:207], v202 offset:6144\n"
"v_mfma_f32_16x16x32_bf16 v[172:175], a[184:187], a[220:223], v[172:175]\n"
"ds_read_b64_tr_b16 a[164:165], v206 offset:8\n"
"v_mfma_f32_16x16x32_bf16 v[188:191], a[188:191], a[220:223], v[188:191]\n"
"s_mov_b32 s22, 896\n"
"ds_read_b64_tr_b16 a[166:167], v206 offset:4136\n"
"v_mfma_f32_16x16x32_bf16 v[184:187], a[188:191], a[216:219], v[184:187]\n"
"s_mov_b32 s23, 3670016\n"
"ds_read_b64_tr_b16 a[168:169], v207 offset:0\n"
"v_mfma_f32_16x16x32_bf16 v[180:183], a[188:191], a[212:215], v[180:183]\n"
"s_mov_b32 s24, 3932160\n"
"ds_read_b64_tr_b16 a[170:171], v207 offset:4128\n"
"v_mfma_f32_16x16x32_bf16 v[176:179], a[188:191], a[208:211], v[176:179]\n"
"s_waitcnt lgkmcnt(7)\n"
"v_mfma_f32_16x16x32_bf16 v[64:67], a[160:163], a[192:195], v[64:67]\n"
"s_waitcnt lgkmcnt(6)\n"
"ds_read_b64_tr_b16 a[172:173], v207 offset:8\n"
"v_mfma_f32_16x16x32_bf16 v[68:71], a[160:163], a[196:199], v[68:71]\n"
"s_waitcnt lgkmcnt(6)\n"
"v_mfma_f32_16x16x32_bf16 v[72:75], a[160:163], a[200:203], v[72:75]\n"
"s_waitcnt lgkmcnt(5)\n"
"ds_read_b64_tr_b16 a[174:175], v207 offset:4136\n"
"v_mfma_f32_16x16x32_bf16 v[76:79], a[160:163], a[204:207], v[76:79]\n"
"s_waitcnt lgkmcnt(4)\n"
"s_add_u32 m0, s31, 98560\n"
"ds_read_b64_tr_b16 a[176:177], v206 offset:512\n"
"v_mfma_f32_16x16x32_bf16 v[92:95], a[164:167], a[204:207], v[92:95]\n"
"buffer_load_dwordx4 v200, s[40:43], s23 offen lds\n"
"v_mfma_f32_16x16x32_bf16 v[88:91], a[164:167], a[200:203], v[88:91]\n"
"ds_read_b64_tr_b16 a[178:179], v206 offset:4640\n"
"v_mfma_f32_16x16x32_bf16 v[84:87], a[164:167], a[196:199], v[84:87]\n"
"s_add_u32 m0, s31, 102688\n"
"ds_read_b128 a[208:211], v203 offset:0\n"
"v_mfma_f32_16x16x32_bf16 v[80:83], a[164:167], a[192:195], v[80:83]\n"
"s_waitcnt lgkmcnt(5)\n"
"buffer_load_dwordx4 v201, s[40:43], s23 offen lds\n"
"v_mfma_f32_16x16x32_bf16 v[96:99], a[168:171], a[192:195], v[96:99]\n"
"ds_read_b64_tr_b16 a[180:181], v206 offset:520\n"
"v_mfma_f32_16x16x32_bf16 v[100:103], a[168:171], a[196:199], v[100:103]\n"
"v_mfma_f32_16x16x32_bf16 v[104:107], a[168:171], a[200:203], v[104:107]\n"
"ds_read_b64_tr_b16 a[182:183], v206 offset:4648\n"
"v_mfma_f32_16x16x32_bf16 v[108:111], a[168:171], a[204:207], v[108:111]\n"
"s_waitcnt lgkmcnt(5)\n"
"s_add_u32 m0, s31, 106816\n"
"ds_read_b128 a[212:215], v203 offset:2048\n"
"v_mfma_f32_16x16x32_bf16 v[124:127], a[172:175], a[204:207], v[124:127]\n"
"buffer_load_dwordx4 v200, s[40:43], s24 offen lds\n"
"v_mfma_f32_16x16x32_bf16 v[120:123], a[172:175], a[200:203], v[120:123]\n"
"ds_read_b64_tr_b16 a[184:185], v207 offset:512\n"
"v_mfma_f32_16x16x32_bf16 v[116:119], a[172:175], a[196:199], v[116:119]\n"
"s_add_u32 m0, s31, 110944\n"
"ds_read_b64_tr_b16 a[186:187], v207 offset:4640\n"
"v_mfma_f32_16x16x32_bf16 v[112:115], a[172:175], a[192:195], v[112:115]\n"
"s_waitcnt lgkmcnt(6)\n"
"buffer_load_dwordx4 v201, s[40:43], s24 offen lds\n"
"v_mfma_f32_16x16x32_bf16 v[128:131], a[176:179], a[192:195], v[128:131]\n"
"ds_read_b128 a[216:219], v203 offset:4096\n"
"v_mfma_f32_16x16x32_bf16 v[132:135], a[176:179], a[196:199], v[132:135]\n"
"v_mfma_f32_16x16x32_bf16 v[136:139], a[176:179], a[200:203], v[136:139]\n"
"ds_read_b64_tr_b16 a[188:189], v207 offset:520\n"
"v_mfma_f32_16x16x32_bf16 v[140:143], a[176:179], a[204:207], v[140:143]\n"
"s_waitcnt lgkmcnt(5)\n"
"s_add_u32 m0, s21, 131072\n"
"ds_read_b64_tr_b16 a[190:191], v207 offset:4648\n"
"v_mfma_f32_16x16x32_bf16 v[156:159], a[180:183], a[204:207], v[156:159]\n"
"buffer_load_dwordx4 v192, s[36:39], s22 offen lds\n"
"v_mfma_f32_16x16x32_bf16 v[152:155], a[180:183], a[200:203], v[152:155]\n"
"ds_read_b128 a[220:223], v203 offset:6144\n"
"v_mfma_f32_16x16x32_bf16 v[148:151], a[180:183], a[196:199], v[148:151]\n"
"s_add_u32 m0, s21, 132096\n"
"ds_read_b64_tr_b16 a[160:161], v206 offset:8256\n"
"v_mfma_f32_16x16x32_bf16 v[144:147], a[180:183], a[192:195], v[144:147]\n"
"s_waitcnt lgkmcnt(5)\n"
"buffer_load_dwordx4 v193, s[36:39], s22 offen lds\n"
"v_mfma_f32_16x16x32_bf16 v[160:163], a[184:187], a[192:195], v[160:163]\n"
"ds_read_b64_tr_b16 a[162:163], v206 offset:12384\n"
"v_mfma_f32_16x16x32_bf16 v[164:167], a[184:187], a[196:199], v[164:167]\n"
"v_mfma_f32_16x16x32_bf16 v[168:171], a[184:187], a[200:203], v[168:171]\n"
"ds_read_b64_tr_b16 a[164:165], v206 offset:8264\n"
"v_mfma_f32_16x16x32_bf16 v[172:175], a[184:187], a[204:207], v[172:175]\n"
"s_waitcnt lgkmcnt(4)\n"
"s_add_u32 m0, s21, 133120\n"
"ds_read_b64_tr_b16 a[166:167], v206 offset:12392\n"
"v_mfma_f32_16x16x32_bf16 v[188:191], a[188:191], a[204:207], v[188:191]\n"
"buffer_load_dwordx4 v194, s[36:39], s22 offen lds\n"
"v_mfma_f32_16x16x32_bf16 v[184:187], a[188:191], a[200:203], v[184:187]\n"
"ds_read_b64_tr_b16 a[168:169], v207 offset:8256\n"
"v_mfma_f32_16x16x32_bf16 v[180:183], a[188:191], a[196:199], v[180:183]\n"
"s_add_u32 m0, s21, 134144\n"
"ds_read_b64_tr_b16 a[170:171], v207 offset:12384\n"
"v_mfma_f32_16x16x32_bf16 v[176:179], a[188:191], a[192:195], v[176:179]\n"
"s_waitcnt lgkmcnt(4)\n"
"buffer_load_dwordx4 v195, s[36:39], s22 offen lds\n"
"v_mfma_f32_16x16x32_bf16 v[64:67], a[160:163], a[208:211], v[64:67]\n"
"ds_read_b64_tr_b16 a[172:173], v207 offset:8264\n"
"v_mfma_f32_16x16x32_bf16 v[68:71], a[160:163], a[212:215], v[68:71]\n"
"v_mfma_f32_16x16x32_bf16 v[72:75], a[160:163], a[216:219], v[72:75]\n"
"ds_read_b64_tr_b16 a[174:175], v207 offset:12392\n"
"v_mfma_f32_16x16x32_bf16 v[76:79], a[160:163], a[220:223], v[76:79]\n"
"s_waitcnt lgkmcnt(4)\n"
"s_add_u32 m0, s21, 135168\n"
"ds_read_b64_tr_b16 a[176:177], v206 offset:8768\n"
"v_mfma_f32_16x16x32_bf16 v[92:95], a[164:167], a[220:223], v[92:95]\n"
"buffer_load_dwordx4 v196, s[36:39], s22 offen lds\n"
"v_mfma_f32_16x16x32_bf16 v[88:91], a[164:167], a[216:219], v[88:91]\n"
"ds_read_b64_tr_b16 a[178:179], v206 offset:12896\n"
"v_mfma_f32_16x16x32_bf16 v[84:87], a[164:167], a[212:215], v[84:87]\n"
"s_add_u32 m0, s21, 136192\n"
"ds_read_b64_tr_b16 a[180:181], v206 offset:8776\n"
"v_mfma_f32_16x16x32_bf16 v[80:83], a[164:167], a[208:211], v[80:83]\n"
"s_waitcnt lgkmcnt(5)\n"
"buffer_load_dwordx4 v197, s[36:39], s22 offen lds\n"
"v_mfma_f32_16x16x32_bf16 v[96:99], a[168:171], a[208:211], v[96:99]\n"
"ds_read_b64_tr_b16 a[182:183], v206 offset:12904\n"
"v_mfma_f32_16x16x32_bf16 v[100:103], a[168:171], a[212:215], v[100:103]\n"
"v_mfma_f32_16x16x32_bf16 v[104:107], a[168:171], a[216:219], v[104:107]\n"
"ds_read_b64_tr_b16 a[184:185], v207 offset:8768\n"
"v_mfma_f32_16x16x32_bf16 v[108:111], a[168:171], a[220:223], v[108:111]\n"
"s_waitcnt lgkmcnt(5)\n"
"s_add_u32 m0, s21, 137216\n"
"ds_read_b64_tr_b16 a[186:187], v207 offset:12896\n"
"v_mfma_f32_16x16x32_bf16 v[124:127], a[172:175], a[220:223], v[124:127]\n"
"buffer_load_dwordx4 v198, s[36:39], s22 offen lds\n"
"v_mfma_f32_16x16x32_bf16 v[120:123], a[172:175], a[216:219], v[120:123]\n"
"ds_read_b64_tr_b16 a[188:189], v207 offset:8776\n"
"v_mfma_f32_16x16x32_bf16 v[116:119], a[172:175], a[212:215], v[116:119]\n"
"s_add_u32 m0, s21, 138240\n"
"ds_read_b64_tr_b16 a[190:191], v207 offset:12904\n"
"v_mfma_f32_16x16x32_bf16 v[112:115], a[172:175], a[208:211], v[112:115]\n"
"s_waitcnt lgkmcnt(6)\n"
"buffer_load_dwordx4 v199, s[36:39], s22 offen lds\n"
"v_mfma_f32_16x16x32_bf16 v[128:131], a[176:179], a[208:211], v[128:131]\n"
"v_mfma_f32_16x16x32_bf16 v[132:135], a[176:179], a[212:215], v[132:135]\n"
"v_mfma_f32_16x16x32_bf16 v[136:139], a[176:179], a[216:219], v[136:139]\n"
"v_mfma_f32_16x16x32_bf16 v[140:143], a[176:179], a[220:223], v[140:143]\n"
"s_waitcnt lgkmcnt(4)\n"
"v_mfma_f32_16x16x32_bf16 v[156:159], a[180:183], a[220:223], v[156:159]\n"
"s_waitcnt vmcnt(12) lgkmcnt(0)\n"
"s_barrier\n"
"v_mfma_f32_16x16x32_bf16 v[152:155], a[180:183], a[216:219], v[152:155]\n"
"ds_read_b128 a[192:195], v202 offset:32768\n"
"v_mfma_f32_16x16x32_bf16 v[148:151], a[180:183], a[212:215], v[148:151]\n"
"ds_read_b64_tr_b16 a[160:161], v206 offset:16512\n"
"v_mfma_f32_16x16x32_bf16 v[144:147], a[180:183], a[208:211], v[144:147]\n"
"ds_read_b64_tr_b16 a[162:163], v206 offset:20640\n"
"v_mfma_f32_16x16x32_bf16 v[160:163], a[184:187], a[208:211], v[160:163]\n"
"ds_read_b128 a[196:199], v202 offset:34816\n"
"v_mfma_f32_16x16x32_bf16 v[164:167], a[184:187], a[212:215], v[164:167]\n"
"ds_read_b128 a[200:203], v202 offset:36864\n"
"v_mfma_f32_16x16x32_bf16 v[168:171], a[184:187], a[216:219], v[168:171]\n"
"ds_read_b128 a[204:207], v202 offset:38912\n"
"v_mfma_f32_16x16x32_bf16 v[172:175], a[184:187], a[220:223], v[172:175]\n"
"ds_read_b64_tr_b16 a[164:165], v206 offset:16520\n"
"v_mfma_f32_16x16x32_bf16 v[188:191], a[188:191], a[220:223], v[188:191]\n"
"ds_read_b64_tr_b16 a[166:167], v206 offset:20648\n"
"v_mfma_f32_16x16x32_bf16 v[184:187], a[188:191], a[216:219], v[184:187]\n"
"ds_read_b64_tr_b16 a[168:169], v207 offset:16512\n"
"v_mfma_f32_16x16x32_bf16 v[180:183], a[188:191], a[212:215], v[180:183]\n"
"ds_read_b64_tr_b16 a[170:171], v207 offset:20640\n"
"v_mfma_f32_16x16x32_bf16 v[176:179], a[188:191], a[208:211], v[176:179]\n"
"s_waitcnt lgkmcnt(7)\n"
"v_mfma_f32_16x16x32_bf16 v[64:67], a[160:163], a[192:195], v[64:67]\n"
"s_waitcnt lgkmcnt(6)\n"
"ds_read_b64_tr_b16 a[172:173], v207 offset:16520\n"
"v_mfma_f32_16x16x32_bf16 v[68:71], a[160:163], a[196:199], v[68:71]\n"
"s_waitcnt lgkmcnt(6)\n"
"v_mfma_f32_16x16x32_bf16 v[72:75], a[160:163], a[200:203], v[72:75]\n"
"s_waitcnt lgkmcnt(5)\n"
"ds_read_b64_tr_b16 a[174:175], v207 offset:20648\n"
"v_mfma_f32_16x16x32_bf16 v[76:79], a[160:163], a[204:207], v[76:79]\n"
"s_waitcnt lgkmcnt(4)\n"
"s_add_u32 m0, s20, 0\n"
"ds_read_b64_tr_b16 a[176:177], v206 offset:17024\n"
"v_mfma_f32_16x16x32_bf16 v[92:95], a[164:167], a[204:207], v[92:95]\n"
"buffer_load_dwordx4 v246, s[44:47], s25 offen nt lds\n"
"v_mfma_f32_16x16x32_bf16 v[88:91], a[164:167], a[200:203], v[88:91]\n"
"s_add_u32 s25, s25, 4096\n"
"ds_read_b64_tr_b16 a[178:179], v206 offset:21152\n"
"v_mfma_f32_16x16x32_bf16 v[84:87], a[164:167], a[196:199], v[84:87]\n"
"s_add_u32 m0, s20, 4096\n"
"ds_read_b128 a[208:211], v203 offset:32768\n"
"v_mfma_f32_16x16x32_bf16 v[80:83], a[164:167], a[192:195], v[80:83]\n"
"s_waitcnt lgkmcnt(5)\n"
"buffer_load_dwordx4 v246, s[44:47], s25 offen nt lds\n"
"v_mfma_f32_16x16x32_bf16 v[96:99], a[168:171], a[192:195], v[96:99]\n"
"s_add_u32 s25, s25, 4096\n"
"ds_read_b64_tr_b16 a[180:181], v206 offset:17032\n"
"v_mfma_f32_16x16x32_bf16 v[100:103], a[168:171], a[196:199], v[100:103]\n"
"v_mfma_f32_16x16x32_bf16 v[104:107], a[168:171], a[200:203], v[104:107]\n"
"ds_read_b64_tr_b16 a[182:183], v206 offset:21160\n"
"v_mfma_f32_16x16x32_bf16 v[108:111], a[168:171], a[204:207], v[108:111]\n"
"s_waitcnt lgkmcnt(5)\n"
"s_add_u32 m0, s20, 8192\n"
"ds_read_b128 a[212:215], v203 offset:34816\n"
"v_mfma_f32_16x16x32_bf16 v[124:127], a[172:175], a[204:207], v[124:127]\n"
"buffer_load_dwordx4 v246, s[44:47], s25 offen nt lds\n"
"v_mfma_f32_16x16x32_bf16 v[120:123], a[172:175], a[200:203], v[120:123]\n"
"s_add_u32 s25, s25, 4096\n"
"ds_read_b64_tr_b16 a[184:185], v207 offset:17024\n"
"v_mfma_f32_16x16x32_bf16 v[116:119], a[172:175], a[196:199], v[116:119]\n"
"s_add_u32 m0, s20, 12288\n"
"ds_read_b64_tr_b16 a[186:187], v207 offset:21152\n"
"v_mfma_f32_16x16x32_bf16 v[112:115], a[172:175], a[192:195], v[112:115]\n"
"s_waitcnt lgkmcnt(6)\n"
"buffer_load_dwordx4 v246, s[44:47], s25 offen nt lds\n"
"v_mfma_f32_16x16x32_bf16 v[128:131], a[176:179], a[192:195], v[128:131]\n"
"s_add_u32 s25, s25, 4096\n"
"ds_read_b128 a[216:219], v203 offset:36864\n"
"v_mfma_f32_16x16x32_bf16 v[132:135], a[176:179], a[196:199], v[132:135]\n"
"v_mfma_f32_16x16x32_bf16 v[136:139], a[176:179], a[200:203], v[136:139]\n"
"ds_read_b64_tr_b16 a[188:189], v207 offset:17032\n"
"v_mfma_f32_16x16x32_bf16 v[140:143], a[176:179], a[204:207], v[140:143]\n"
"s_waitcnt lgkmcnt(5)\n"
"s_add_u32 m0, s20, 24576\n"
"ds_read_b64_tr_b16 a[190:191], v207 offset:21160\n"
"v_mfma_f32_16x16x32_bf16 v[156:159], a[180:183], a[204:207], v[156:159]\n"
"buffer_load_dwordx4 v247, s[48:51], s26 offen nt lds\n"
"v_mfma_f32_16x16x32_bf16 v[152:155], a[180:183], a[200:203], v[152:155]\n"
"s_add_u32 s26, s26, 4096\n"
"ds_read_b128 a[220:223], v203 offset:38912\n"
"v_mfma_f32_16x16x32_bf16 v[148:151], a[180:183], a[196:199], v[148:151]\n"
"s_add_u32 m0, s20, 28672\n"
"ds_read_b64_tr_b16 a[160:161], v206 offset:24768\n"
"v_mfma_f32_16x16x32_bf16 v[144:147], a[180:183], a[192:195], v[144:147]\n"
"s_waitcnt lgkmcnt(5)\n"
"buffer_load_dwordx4 v247, s[48:51], s26 offen nt lds\n"
"v_mfma_f32_16x16x32_bf16 v[160:163], a[184:187], a[192:195], v[160:163]\n"
"s_add_u32 s26, s26, 4096\n"
"ds_read_b64_tr_b16 a[162:163], v206 offset:28896\n"
"v_mfma_f32_16x16x32_bf16 v[164:167], a[184:187], a[196:199], v[164:167]\n"
"v_mfma_f32_16x16x32_bf16 v[168:171], a[184:187], a[200:203], v[168:171]\n"
"ds_read_b64_tr_b16 a[164:165], v206 offset:24776\n"
"v_mfma_f32_16x16x32_bf16 v[172:175], a[184:187], a[204:207], v[172:175]\n"
"s_waitcnt lgkmcnt(4)\n"
"ds_read_b64_tr_b16 a[166:167], v206 offset:28904\n"
"v_mfma_f32_16x16x32_bf16 v[188:191], a[188:191], a[204:207], v[188:191]\n"
"v_mfma_f32_16x16x32_bf16 v[184:187], a[188:191], a[200:203], v[184:187]\n"
"ds_read_b64_tr_b16 a[168:169], v207 offset:24768\n"
"v_mfma_f32_16x16x32_bf16 v[180:183], a[188:191], a[196:199], v[180:183]\n"
"ds_read_b64_tr_b16 a[170:171], v207 offset:28896\n"
"v_mfma_f32_16x16x32_bf16 v[176:179], a[188:191], a[192:195], v[176:179]\n"
"s_waitcnt lgkmcnt(4)\n"
"v_mfma_f32_16x16x32_bf16 v[64:67], a[160:163], a[208:211], v[64:67]\n"
"ds_read_b64_tr_b16 a[172:173], v207 offset:24776\n"
"v_mfma_f32_16x16x32_bf16 v[68:71], a[160:163], a[212:215], v[68:71]\n"
"v_mfma_f32_16x16x32_bf16 v[72:75], a[160:163], a[216:219], v[72:75]\n"
"ds_read_b64_tr_b16 a[174:175], v207 offset:28904\n"
"v_mfma_f32_16x16x32_bf16 v[76:79], a[160:163], a[220:223], v[76:79]\n"
"s_waitcnt lgkmcnt(4)\n"
"ds_read_b64_tr_b16 a[176:177], v206 offset:25280\n"
"v_mfma_f32_16x16x32_bf16 v[92:95], a[164:167], a[220:223], v[92:95]\n"
"v_mfma_f32_16x16x32_bf16 v[88:91], a[164:167], a[216:219], v[88:91]\n"
"ds_read_b64_tr_b16 a[178:179], v206 offset:29408\n"
"v_mfma_f32_16x16x32_bf16 v[84:87], a[164:167], a[212:215], v[84:87]\n"
"ds_read_b64_tr_b16 a[180:181], v206 offset:25288\n"
"v_mfma_f32_16x16x32_bf16 v[80:83], a[164:167], a[208:211], v[80:83]\n"
"s_waitcnt lgkmcnt(5)\n"
"v_mfma_f32_16x16x32_bf16 v[96:99], a[168:171], a[208:211], v[96:99]\n"
"ds_read_b64_tr_b16 a[182:183], v206 offset:29416\n"
"v_mfma_f32_16x16x32_bf16 v[100:103], a[168:171], a[212:215], v[100:103]\n"
"v_mfma_f32_16x16x32_bf16 v[104:107], a[168:171], a[216:219], v[104:107]\n"
"ds_read_b64_tr_b16 a[184:185], v207 offset:25280\n"
"v_mfma_f32_16x16x32_bf16 v[108:111], a[168:171], a[220:223], v[108:111]\n"
"s_waitcnt lgkmcnt(5)\n"
"ds_read_b64_tr_b16 a[186:187], v207 offset:29408\n"
"v_mfma_f32_16x16x32_bf16 v[124:127], a[172:175], a[220:223], v[124:127]\n"
"v_mfma_f32_16x16x32_bf16 v[120:123], a[172:175], a[216:219], v[120:123]\n"
"ds_read_b64_tr_b16 a[188:189], v207 offset:25288\n"
"v_mfma_f32_16x16x32_bf16 v[116:119], a[172:175], a[212:215], v[116:119]\n"
"ds_read_b64_tr_b16 a[190:191], v207 offset:29416\n"
"v_mfma_f32_16x16x32_bf16 v[112:115], a[172:175], a[208:211], v[112:115]\n"
"s_waitcnt lgkmcnt(6)\n"
"v_mfma_f32_16x16x32_bf16 v[128:131], a[176:179], a[208:211], v[128:131]\n"
"v_mfma_f32_16x16x32_bf16 v[132:135], a[176:179], a[212:215], v[132:135]\n"
"v_mfma_f32_16x16x32_bf16 v[136:139], a[176:179], a[216:219], v[136:139]\n"
"v_mfma_f32_16x16x32_bf16 v[140:143], a[176:179], a[220:223], v[140:143]\n"
"s_waitcnt lgkmcnt(4)\n"
"v_mfma_f32_16x16x32_bf16 v[156:159], a[180:183], a[220:223], v[156:159]\n"
"s_waitcnt vmcnt(6) lgkmcnt(0)\n"
"s_barrier\n"
"v_mfma_f32_16x16x32_bf16 v[152:155], a[180:183], a[216:219], v[152:155]\n"
"ds_read_b128 a[192:195], v204 offset:0\n"
"v_mfma_f32_16x16x32_bf16 v[148:151], a[180:183], a[212:215], v[148:151]\n"
"ds_read_b64_tr_b16 a[160:161], v206 offset:33024\n"
"v_mfma_f32_16x16x32_bf16 v[144:147], a[180:183], a[208:211], v[144:147]\n"
"ds_read_b64_tr_b16 a[162:163], v206 offset:37152\n"
"v_mfma_f32_16x16x32_bf16 v[160:163], a[184:187], a[208:211], v[160:163]\n"
"ds_read_b128 a[196:199], v204 offset:2048\n"
"ds_read_b128 a[236:239], v205 offset:6144\n"
"v_mfma_f32_16x16x32_bf16 v[164:167], a[184:187], a[212:215], v[164:167]\n"
"ds_read_b128 a[200:203], v204 offset:4096\n"
"v_mfma_f32_16x16x32_bf16 v[168:171], a[184:187], a[216:219], v[168:171]\n"
"ds_read_b128 a[204:207], v204 offset:6144\n"
"ds_read_b128 a[232:235], v205 offset:4096\n"
"v_mfma_f32_16x16x32_bf16 v[172:175], a[184:187], a[220:223], v[172:175]\n"
"ds_read_b64_tr_b16 a[164:165], v206 offset:41280\n"
"v_mfma_f32_16x16x32_bf16 v[188:191], a[188:191], a[220:223], v[188:191]\n"
"ds_read_b64_tr_b16 a[166:167], v206 offset:45408\n"
"v_mfma_f32_16x16x32_bf16 v[184:187], a[188:191], a[216:219], v[184:187]\n"
"ds_read_b64_tr_b16 a[168:169], v206 offset:33032\n"
"v_mfma_f32_16x16x32_bf16 v[180:183], a[188:191], a[212:215], v[180:183]\n"
"ds_read_b64_tr_b16 a[170:171], v206 offset:37160\n"
"v_mfma_f32_16x16x32_bf16 v[176:179], a[188:191], a[208:211], v[176:179]\n"
"s_waitcnt lgkmcnt(9)\n"
"ds_read_b128 a[228:231], v205 offset:2048\n"
"v_mfma_f32_16x16x32_bf16 v[64:67], a[160:163], a[192:195], v[64:67]\n"
"s_waitcnt lgkmcnt(9)\n"
"ds_read_b64_tr_b16 a[172:173], v206 offset:41288\n"
"v_mfma_f32_16x16x32_bf16 v[68:71], a[160:163], a[196:199], v[68:71]\n"
"s_waitcnt lgkmcnt(8)\n"
"ds_read_b128 a[224:227], v205 offset:0\n"
"v_mfma_f32_16x16x32_bf16 v[72:75], a[160:163], a[200:203], v[72:75]\n"
"s_waitcnt lgkmcnt(8)\n"
"ds_read_b64_tr_b16 a[174:175], v206 offset:45416\n"
"v_mfma_f32_16x16x32_bf16 v[76:79], a[160:163], a[204:207], v[76:79]\n"
"s_waitcnt lgkmcnt(6)\n"
"s_add_u32 m0, s20, 65536\n"
"ds_read_b64_tr_b16 a[176:177], v207 offset:33024\n"
"v_mfma_f32_16x16x32_bf16 v[76:79], a[164:167], a[236:239], v[76:79]\n"
"buffer_load_dwordx4 v246, s[44:47], s25 offen nt lds\n"
"v_mfma_f32_16x16x32_bf16 v[72:75], a[164:167], a[232:235], v[72:75]\n"
"s_waitcnt lgkmcnt(4)\n"
"s_add_u32 s25, s25, 4096\n"
"ds_read_b64_tr_b16 a[178:179], v207 offset:37152\n"
"v_mfma_f32_16x16x32_bf16 v[68:71], a[164:167], a[228:231], v[68:71]\n"
"s_waitcnt lgkmcnt(3)\n"
"ds_read_b64_tr_b16 a[180:181], v207 offset:41280\n"
"v_mfma_f32_16x16x32_bf16 v[64:67], a[164:167], a[224:227], v[64:67]\n"
"v_mfma_f32_16x16x32_bf16 v[80:83], a[168:171], a[192:195], v[80:83]\n"
"ds_read_b64_tr_b16 a[182:183], v207 offset:45408\n"
"v_mfma_f32_16x16x32_bf16 v[84:87], a[168:171], a[196:199], v[84:87]\n"
"v_mfma_f32_16x16x32_bf16 v[88:91], a[168:171], a[200:203], v[88:91]\n"
"ds_read_b64_tr_b16 a[184:185], v207 offset:33032\n"
"v_mfma_f32_16x16x32_bf16 v[92:95], a[168:171], a[204:207], v[92:95]\n"
"s_waitcnt lgkmcnt(5)\n"
"ds_read_b64_tr_b16 a[186:187], v207 offset:37160\n"
"v_mfma_f32_16x16x32_bf16 v[92:95], a[172:175], a[236:239], v[92:95]\n"
"v_mfma_f32_16x16x32_bf16 v[88:91], a[172:175], a[232:235], v[88:91]\n"
"ds_read_b64_tr_b16 a[188:189], v207 offset:41288\n"
"v_mfma_f32_16x16x32_bf16 v[84:87], a[172:175], a[228:231], v[84:87]\n"
"s_add_u32 m0, s20, 69632\n"
"ds_read_b64_tr_b16 a[190:191], v207 offset:45416\n"
"v_mfma_f32_16x16x32_bf16 v[80:83], a[172:175], a[224:227], v[80:83]\n"
"s_waitcnt lgkmcnt(6)\n"
"buffer_load_dwordx4 v246, s[44:47], s25 offen nt lds\n"
"v_mfma_f32_16x16x32_bf16 v[96:99], a[176:179], a[192:195], v[96:99]\n"
"s_add_u32 s25, s25, 4096\n"
"ds_read_b64_tr_b16 a[160:161], v206 offset:33536\n"
"v_mfma_f32_16x16x32_bf16 v[100:103], a[176:179], a[196:199], v[100:103]\n"
"v_mfma_f32_16x16x32_bf16 v[104:107], a[176:179], a[200:203], v[104:107]\n"
"ds_read_b64_tr_b16 a[162:163], v206 offset:37664\n"
"v_mfma_f32_16x16x32_bf16 v[108:111], a[176:179], a[204:207], v[108:111]\n"
"s_waitcnt lgkmcnt(6)\n"
"ds_read_b64_tr_b16 a[164:165], v206 offset:41792\n"
"v_mfma_f32_16x16x32_bf16 v[108:111], a[180:183], a[236:239], v[108:111]\n"
"v_mfma_f32_16x16x32_bf16 v[104:107], a[180:183], a[232:235], v[104:107]\n"
"ds_read_b64_tr_b16 a[166:167], v206 offset:45920\n"
"v_mul_f32 v64, s30, v64\n"
"v_mfma_f32_16x16x32_bf16 v[100:103], a[180:183], a[228:231], v[100:103]\n"
"ds_read_b64_tr_b16 a[168:169], v206 offset:33544\n"
"v_mul_f32 v65, s30, v65\n"
"v_mfma_f32_16x16x32_bf16 v[96:99], a[180:183], a[224:227], v[96:99]\n"
"s_waitcnt lgkmcnt(7)\n"
"v_mul_f32 v66, s30, v66\n"
"v_mul_f32 v67, s30, v67\n"
"v_mfma_f32_16x16x32_bf16 v[112:115], a[184:187], a[192:195], v[112:115]\n"
"ds_read_b64_tr_b16 a[170:171], v206 offset:37672\n"
"v_mul_f32 v80, s30, v80\n"
"v_mfma_f32_16x16x32_bf16 v[116:119], a[184:187], a[196:199], v[116:119]\n"
"v_mul_f32 v81, s30, v81\n"
"v_mul_f32 v82, s30, v82\n"
"v_mfma_f32_16x16x32_bf16 v[120:123], a[184:187], a[200:203], v[120:123]\n"
"ds_read_b64_tr_b16 a[172:173], v206 offset:41800\n"
"v_mul_f32 v83, s30, v83\n"
"v_mfma_f32_16x16x32_bf16 v[124:127], a[184:187], a[204:207], v[124:127]\n"
"s_waitcnt lgkmcnt(7)\n"
"s_add_u32 m0, s20, 81920\n"
"ds_read_b64_tr_b16 a[174:175], v206 offset:45928\n"
"v_cvt_pk_bf16_f32 v0, v64, v65\n"
"v_mfma_f32_16x16x32_bf16 v[124:127], a[188:191], a[236:239], v[124:127]\n"
"buffer_load_dwordx4 v247, s[48:51], s26 offen nt lds\n"
"v_mfma_f32_16x16x32_bf16 v[120:123], a[188:191], a[232:235], v[120:123]\n"
"s_add_u32 s26, s26, 4096\n"
"ds_read_b64_tr_b16 a[176:177], v207 offset:33536\n"
"v_cvt_pk_bf16_f32 v1, v66, v67\n"
"v_mfma_f32_16x16x32_bf16 v[116:119], a[188:191], a[228:231], v[116:119]\n"
"ds_read_b64_tr_b16 a[178:179], v207 offset:37664\n"
"v_cvt_pk_bf16_f32 v2, v80, v81\n"
"v_mfma_f32_16x16x32_bf16 v[112:115], a[188:191], a[224:227], v[112:115]\n"
"s_waitcnt lgkmcnt(8)\n"
"v_cvt_pk_bf16_f32 v3, v82, v83\n"
"v_mul_f32 v68, s30, v68\n"
"v_mfma_f32_16x16x32_bf16 v[128:131], a[160:163], a[192:195], v[128:131]\n"
"ds_read_b64_tr_b16 a[180:181], v207 offset:41792\n"
"v_mul_f32 v69, s30, v69\n"
"v_mfma_f32_16x16x32_bf16 v[132:135], a[160:163], a[196:199], v[132:135]\n"
"v_mul_f32 v70, s30, v70\n"
"v_mul_f32 v71, s30, v71\n"
"v_mfma_f32_16x16x32_bf16 v[136:139], a[160:163], a[200:203], v[136:139]\n"
"ds_read_b64_tr_b16 a[182:183], v207 offset:45920\n"
"v_mul_f32 v84, s30, v84\n"
"v_mfma_f32_16x16x32_bf16 v[140:143], a[160:163], a[204:207], v[140:143]\n"
"s_waitcnt lgkmcnt(8)\n"
"ds_read_b64_tr_b16 a[184:185], v207 offset:33544\n"
"v_mul_f32 v85, s30, v85\n"
"v_mfma_f32_16x16x32_bf16 v[140:143], a[164:167], a[236:239], v[140:143]\n"
"v_mul_f32 v86, s30, v86\n"
"v_mul_f32 v87, s30, v87\n"
"v_mfma_f32_16x16x32_bf16 v[136:139], a[164:167], a[232:235], v[136:139]\n"
"ds_read_b64_tr_b16 a[186:187], v207 offset:37672\n"
"v_cvt_pk_bf16_f32 v4, v68, v69\n"
"v_mfma_f32_16x16x32_bf16 v[132:135], a[164:167], a[228:231], v[132:135]\n"
"s_add_u32 m0, s20, 86016\n"
"ds_read_b64_tr_b16 a[188:189], v207 offset:41800\n"
"v_cvt_pk_bf16_f32 v5, v70, v71\n"
"v_mfma_f32_16x16x32_bf16 v[128:131], a[164:167], a[224:227], v[128:131]\n"
"s_waitcnt lgkmcnt(9)\n"
"buffer_load_dwordx4 v247, s[48:51], s26 offen nt lds\n"
"v_mfma_f32_16x16x32_bf16 v[144:147], a[168:171], a[192:195], v[144:147]\n"
"s_add_u32 s26, s26, 4096\n"
"ds_read_b64_tr_b16 a[190:191], v207 offset:45928\n"
"v_cvt_pk_bf16_f32 v6, v84, v85\n"
"v_mfma_f32_16x16x32_bf16 v[148:151], a[168:171], a[196:199], v[148:151]\n"
"v_cvt_pk_bf16_f32 v7, v86, v87\n"
"v_mul_f32 v72, s30, v72\n"
"v_mfma_f32_16x16x32_bf16 v[152:155], a[168:171], a[200:203], v[152:155]\n"
"v_mul_f32 v73, s30, v73\n"
"v_mul_f32 v74, s30, v74\n"
"v_mfma_f32_16x16x32_bf16 v[156:159], a[168:171], a[204:207], v[156:159]\n"
"s_waitcnt lgkmcnt(8)\n"
"v_mul_f32 v75, s30, v75\n"
"v_mul_f32 v88, s30, v88\n"
"v_mfma_f32_16x16x32_bf16 v[156:159], a[172:175], a[236:239], v[156:159]\n"
"v_mul_f32 v89, s30, v89\n"
"v_mul_f32 v90, s30, v90\n"
"v_mfma_f32_16x16x32_bf16 v[152:155], a[172:175], a[232:235], v[152:155]\n"
"v_mul_f32 v91, s30, v91\n"
"v_cvt_pk_bf16_f32 v8, v72, v73\n"
"v_mfma_f32_16x16x32_bf16 v[148:151], a[172:175], a[228:231], v[148:151]\n"
"v_cvt_pk_bf16_f32 v9, v74, v75\n"
"v_cvt_pk_bf16_f32 v10, v88, v89\n"
"v_mfma_f32_16x16x32_bf16 v[144:147], a[172:175], a[224:227], v[144:147]\n"
"s_waitcnt lgkmcnt(6)\n"
"v_cvt_pk_bf16_f32 v11, v90, v91\n"
"v_mul_f32 v76, s30, v76\n"
"v_mfma_f32_16x16x32_bf16 v[160:163], a[176:179], a[192:195], v[160:163]\n"
"s_memrealtime s[82:83]\n"
"v_mul_f32 v77, s30, v77\n"
"v_mul_f32 v78, s30, v78\n"
"v_mfma_f32_16x16x32_bf16 v[164:167], a[176:179], a[196:199], v[164:167]\n"
"v_mul_f32 v79, s30, v79\n"
"v_mul_f32 v92, s30, v92\n"
"v_mfma_f32_16x16x32_bf16 v[168:171], a[176:179], a[200:203], v[168:171]\n"
"v_mul_f32 v93, s30, v93\n"
"v_mul_f32 v94, s30, v94\n"
"v_mfma_f32_16x16x32_bf16 v[172:175], a[176:179], a[204:207], v[172:175]\n"
"s_waitcnt lgkmcnt(4)\n"
"v_mul_f32 v95, s30, v95\n"
"v_cvt_pk_bf16_f32 v12, v76, v77\n"
"v_mfma_f32_16x16x32_bf16 v[172:175], a[180:183], a[236:239], v[172:175]\n"
"s_waitcnt vmcnt(4) lgkmcnt(0)\n"
"s_barrier\n"
"v_mfma_f32_16x16x32_bf16 v[168:171], a[180:183], a[232:235], v[168:171]\n"
"s_sub_u32 s83, s82, s18\n"
"s_add_u32 s83, s83, 70\n"
"v_cvt_pk_bf16_f32 v13, v78, v79\n"
"v_cvt_pk_bf16_f32 v14, v92, v93\n"
"v_mfma_f32_16x16x32_bf16 v[164:167], a[180:183], a[228:231], v[164:167]\n"
"s_mov_b32 s22, 65536\n"
"v_cvt_pk_bf16_f32 v15, v94, v95\n"
"v_mul_f32 v96, s30, v96\n"
"v_mfma_f32_16x16x32_bf16 v[160:163], a[180:183], a[224:227], v[160:163]\n"
"s_mov_b32 s24, 0\n"
"ds_read_b128 a[160:163], v240 offset:0\n"
"v_mul_f32 v97, s30, v97\n"
"v_mfma_f32_16x16x32_bf16 v[176:179], a[184:187], a[192:195], v[176:179]\n"
"s_min_i32 s83, s83, 0\n"
"v_mul_f32 v98, s30, v98\n"
"v_mul_f32 v99, s30, v99\n"
"v_mfma_f32_16x16x32_bf16 v[180:183], a[184:187], a[196:199], v[180:183]\n"
"s_mov_b32 s70, 0\n"
"s_add_u32 m0, s20, 73728\n"
"v_mul_f32 v112, s30, v112\n"
"v_mul_f32 v113, s30, v113\n"
"v_mfma_f32_16x16x32_bf16 v[184:187], a[184:187], a[200:203], v[184:187]\n"
"s_add_u32 s18, s18, s83\n"
"buffer_load_dwordx4 v246, s[44:47], s25 offen nt lds\n"
"v_mfma_f32_16x16x32_bf16 v[188:191], a[184:187], a[204:207], v[188:191]\n"
"s_mov_b32 s19, s18\n"
"s_add_u32 s25, s25, 4096\n"
"ds_read_b128 a[164:167], v240 offset:4096\n"
"v_mul_f32 v114, s30, v114\n"
"v_mfma_f32_16x16x32_bf16 v[188:191], a[188:191], a[236:239], v[188:191]\n"
"s_mov_b32 s36, 0x64100158\n"
"v_mul_f32 v115, s30, v115\n"
"v_cvt_pk_bf16_f32 v16, v96, v97\n"
"v_mfma_f32_16x16x32_bf16 v[184:187], a[188:191], a[232:235], v[184:187]\n"
"s_mov_b32 s37, 0x8888877\n"
"s_add_u32 m0, s20, 77824\n"
"v_cvt_pk_bf16_f32 v17, v98, v99\n"
"v_cvt_pk_bf16_f32 v18, v112, v113\n"
"v_mfma_f32_16x16x32_bf16 v[180:183], a[188:191], a[228:231], v[180:183]\n"
"buffer_load_dwordx4 v246, s[44:47], s25 offen nt lds\n"
"v_mfma_f32_16x16x32_bf16 v[176:179], a[188:191], a[224:227], v[176:179]\n"
"s_waitcnt lgkmcnt(1)\n"
"s_add_u32 s25, s25, 4096\n"
"ds_read_b128 a[168:171], v241 offset:0\n"
"v_cvt_pk_bf16_f32 v19, v114, v115\n"
"v_mfma_f32_16x16x32_bf16 v[64:67], a[160:163], v[0:3], 0\n"
"v_mul_f32 v100, s30, v100\n"
"v_mul_f32 v101, s30, v101\n"
"v_mfma_f32_16x16x32_bf16 v[68:71], a[160:163], v[4:7], 0\n"
"s_add_u32 m0, s20, 90112\n"
"v_mul_f32 v102, s30, v102\n"
"v_mul_f32 v103, s30, v103\n"
"v_mfma_f32_16x16x32_bf16 v[72:75], a[160:163], v[8:11], 0\n"
"buffer_load_dwordx4 v247, s[48:51], s26 offen nt lds\n"
"v_mfma_f32_16x16x32_bf16 v[76:79], a[160:163], v[12:15], 0\n"
"s_waitcnt lgkmcnt(1)\n"
"s_add_u32 s26, s26, 4096\n"
"ds_read_b128 a[172:175], v241 offset:4096\n"
"v_mul_f32 v116, s30, v116\n"
"v_mfma_f32_16x16x32_bf16 v[92:95], a[164:167], v[12:15], 0\n"
"v_mul_f32 v117, s30, v117\n"
"v_mul_f32 v118, s30, v118\n"
"v_mfma_f32_16x16x32_bf16 v[88:91], a[164:167], v[8:11], 0\n"
"s_add_u32 m0, s20, 94208\n"
"v_mul_f32 v119, s30, v119\n"
"v_cvt_pk_bf16_f32 v20, v100, v101\n"
"v_mfma_f32_16x16x32_bf16 v[84:87], a[164:167], v[4:7], 0\n"
"buffer_load_dwordx4 v247, s[48:51], s26 offen nt lds\n"
"v_mfma_f32_16x16x32_bf16 v[80:83], a[164:167], v[0:3], 0\n"
"s_waitcnt lgkmcnt(1)\n"
"s_add_u32 s26, s26, 4096\n"
"ds_read_b128 a[176:179], v240 offset:512\n"
"v_cvt_pk_bf16_f32 v21, v102, v103\n"
"v_cvt_pk_bf16_f32 v22, v116, v117\n"
"v_cvt_pk_bf16_f32 v23, v118, v119\n"
"v_mfma_f32_16x16x32_bf16 v[64:67], a[168:171], v[16:19], v[64:67]\n"
"v_mul_f32 v104, s30, v104\n"
"v_mul_f32 v105, s30, v105\n"
"v_mul_f32 v106, s30, v106\n"
"v_mul_f32 v107, s30, v107\n"
"v_mul_f32 v120, s30, v120\n"
"v_mul_f32 v121, s30, v121\n"
"v_mul_f32 v122, s30, v122\n"
"v_mul_f32 v123, s30, v123\n"
"v_cvt_pk_bf16_f32 v24, v104, v105\n"
"v_cvt_pk_bf16_f32 v25, v106, v107\n"
"v_cvt_pk_bf16_f32 v26, v120, v121\n"
"v_cvt_pk_bf16_f32 v27, v122, v123\n"
"v_mfma_f32_16x16x32_bf16 v[68:71], a[168:171], v[20:23], v[68:71]\n"
"s_add_u32 m0, s20, 32768\n"
"v_mul_f32 v108, s30, v108\n"
"v_mul_f32 v109, s30, v109\n"
"v_mul_f32 v110, s30, v110\n"
"v_mul_f32 v111, s30, v111\n"
"v_mul_f32 v124, s30, v124\n"
"v_mul_f32 v125, s30, v125\n"
"v_mul_f32 v126, s30, v126\n"
"v_mul_f32 v127, s30, v127\n"
"v_cvt_pk_bf16_f32 v28, v108, v109\n"
"v_cvt_pk_bf16_f32 v29, v110, v111\n"
"v_cvt_pk_bf16_f32 v30, v124, v125\n"
"v_cvt_pk_bf16_f32 v31, v126, v127\n"
"v_mfma_f32_16x16x32_bf16 v[72:75], a[168:171], v[24:27], v[72:75]\n"
"buffer_load_dwordx4 v246, s[44:47], s25 offen nt lds\n"
"v_mfma_f32_16x16x32_bf16 v[76:79], a[168:171], v[28:31], v[76:79]\n"
"s_waitcnt lgkmcnt(1)\n"
"s_add_u32 s25, s25, 4096\n"
"ds_read_b128 a[180:183], v240 offset:4608\n"
"v_mul_f32 v128, s30, v128\n"
"v_mfma_f32_16x16x32_bf16 v[92:95], a[172:175], v[28:31], v[92:95]\n"
"v_mul_f32 v129, s30, v129\n"
"v_mul_f32 v130, s30, v130\n"
"v_mfma_f32_16x16x32_bf16 v[88:91], a[172:175], v[24:27], v[88:91]\n"
"s_add_u32 m0, s20, 36864\n"
"v_mul_f32 v131, s30, v131\n"
"v_mul_f32 v144, s30, v144\n"
"v_mfma_f32_16x16x32_bf16 v[84:87], a[172:175], v[20:23], v[84:87]\n"
"buffer_load_dwordx4 v246, s[44:47], s25 offen nt lds\n"
"v_mul_f32 v145, s30, v145\n"
"v_mul_f32 v146, s30, v146\n"
"v_mul_f32 v147, s30, v147\n"
"v_cvt_pk_bf16_f32 v32, v128, v129\n"
"v_cvt_pk_bf16_f32 v33, v130, v131\n"
"v_cvt_pk_bf16_f32 v34, v144, v145\n"
"v_cvt_pk_bf16_f32 v35, v146, v147\n"
"v_mfma_f32_16x16x32_bf16 v[80:83], a[172:175], v[16:19], v[80:83]\n"
"s_waitcnt lgkmcnt(1)\n"
"s_add_u32 s25, s25, 4096\n"
"ds_read_b128 a[184:187], v241 offset:512\n"
"v_mul_f32 v132, s30, v132\n"
"v_mul_f32 v133, s30, v133\n"
"v_mul_f32 v134, s30, v134\n"
"v_mul_f32 v135, s30, v135\n"
"v_mul_f32 v148, s30, v148\n"
"v_mul_f32 v149, s30, v149\n"
"v_mul_f32 v150, s30, v150\n"
"v_mul_f32 v151, s30, v151\n"
"v_cvt_pk_bf16_f32 v36, v132, v133\n"
"v_cvt_pk_bf16_f32 v37, v134, v135\n"
"v_cvt_pk_bf16_f32 v38, v148, v149\n"
"v_cvt_pk_bf16_f32 v39, v150, v151\n"
"v_mfma_f32_16x16x32_bf16 v[64:67], a[176:179], v[32:35], v[64:67]\n"
"v_mul_f32 v136, s30, v136\n"
"v_mul_f32 v137, s30, v137\n"
"v_mul_f32 v138, s30, v138\n"
"v_mul_f32 v139, s30, v139\n"
"v_mul_f32 v152, s30, v152\n"
"v_mul_f32 v153, s30, v153\n"
"v_mul_f32 v154, s30, v154\n"
"v_mul_f32 v155, s30, v155\n"
"v_cvt_pk_bf16_f32 v40, v136, v137\n"
"v_cvt_pk_bf16_f32 v41, v138, v139\n"
"v_cvt_pk_bf16_f32 v42, v152, v153\n"
"v_cvt_pk_bf16_f32 v43, v154, v155\n"
"v_mfma_f32_16x16x32_bf16 v[68:71], a[176:179], v[36:39], v[68:71]\n"
"s_add_u32 m0, s20, 49152\n"
"v_mul_f32 v140, s30, v140\n"
"v_mul_f32 v141, s30, v141\n"
"v_mul_f32 v142, s30, v142\n"
"v_mul_f32 v143, s30, v143\n"
"v_mul_f32 v156, s30, v156\n"
"v_mul_f32 v157, s30, v157\n"
"v_mul_f32 v158, s30, v158\n"
"v_mul_f32 v159, s30, v159\n"
"v_cvt_pk_bf16_f32 v44, v140, v141\n"
"v_cvt_pk_bf16_f32 v45, v142, v143\n"
"v_cvt_pk_bf16_f32 v46, v156, v157\n"
"v_cvt_pk_bf16_f32 v47, v158, v159\n"
"v_mfma_f32_16x16x32_bf16 v[72:75], a[176:179], v[40:43], v[72:75]\n"
"buffer_load_dwordx4 v247, s[48:51], s26 offen nt lds\n"
"v_mfma_f32_16x16x32_bf16 v[76:79], a[176:179], v[44:47], v[76:79]\n"
"s_waitcnt lgkmcnt(1)\n"
"s_add_u32 s26, s26, 4096\n"
"ds_read_b128 a[188:191], v241 offset:4608\n"
"v_mul_f32 v160, s30, v160\n"
"v_mfma_f32_16x16x32_bf16 v[92:95], a[180:183], v[44:47], v[92:95]\n"
"v_mul_f32 v161, s30, v161\n"
"v_mul_f32 v162, s30, v162\n"
"v_mfma_f32_16x16x32_bf16 v[88:91], a[180:183], v[40:43], v[88:91]\n"
"s_add_u32 m0, s20, 53248\n"
"v_mul_f32 v163, s30, v163\n"
"v_mul_f32 v176, s30, v176\n"
"v_mfma_f32_16x16x32_bf16 v[84:87], a[180:183], v[36:39], v[84:87]\n"
"buffer_load_dwordx4 v247, s[48:51], s26 offen nt lds\n"
"v_mul_f32 v177, s30, v177\n"
"v_mul_f32 v178, s30, v178\n"
"v_mul_f32 v179, s30, v179\n"
"v_cvt_pk_bf16_f32 v48, v160, v161\n"
"v_cvt_pk_bf16_f32 v49, v162, v163\n"
"v_cvt_pk_bf16_f32 v50, v176, v177\n"
"v_cvt_pk_bf16_f32 v51, v178, v179\n"
"v_mfma_f32_16x16x32_bf16 v[80:83], a[180:183], v[32:35], v[80:83]\n"
"s_waitcnt lgkmcnt(1)\n"
"s_add_u32 s26, s26, 4096\n"
"ds_read_b128 a[160:163], v240 offset:8192\n"
"v_mul_f32 v164, s30, v164\n"
"v_mul_f32 v165, s30, v165\n"
"v_mul_f32 v166, s30, v166\n"
"v_mul_f32 v167, s30, v167\n"
"v_mul_f32 v180, s30, v180\n"
"v_mul_f32 v181, s30, v181\n"
"v_mul_f32 v182, s30, v182\n"
"v_mul_f32 v183, s30, v183\n"
"v_cvt_pk_bf16_f32 v52, v164, v165\n"
"v_cvt_pk_bf16_f32 v53, v166, v167\n"
"v_cvt_pk_bf16_f32 v54, v180, v181\n"
"v_cvt_pk_bf16_f32 v55, v182, v183\n"
"v_mfma_f32_16x16x32_bf16 v[64:67], a[184:187], v[48:51], v[64:67]\n"
"v_mul_f32 v168, s30, v168\n"
"v_mul_f32 v169, s30, v169\n"
"v_mul_f32 v170, s30, v170\n"
"v_mul_f32 v171, s30, v171\n"
"v_mul_f32 v184, s30, v184\n"
"v_mul_f32 v185, s30, v185\n"
"v_mul_f32 v186, s30, v186\n"
"v_mul_f32 v187, s30, v187\n"
"v_cvt_pk_bf16_f32 v56, v168, v169\n"
"v_cvt_pk_bf16_f32 v57, v170, v171\n"
"v_cvt_pk_bf16_f32 v58, v184, v185\n"
"v_cvt_pk_bf16_f32 v59, v186, v187\n"
"v_mfma_f32_16x16x32_bf16 v[68:71], a[184:187], v[52:55], v[68:71]\n"
"s_add_u32 m0, s20, 40960\n"
"v_mul_f32 v172, s30, v172\n"
"v_mul_f32 v173, s30, v173\n"
"v_mul_f32 v174, s30, v174\n"
"v_mul_f32 v175, s30, v175\n"
"v_mul_f32 v188, s30, v188\n"
"v_mul_f32 v189, s30, v189\n"
"v_mul_f32 v190, s30, v190\n"
"v_mul_f32 v191, s30, v191\n"
"v_cvt_pk_bf16_f32 v60, v172, v173\n"
"v_cvt_pk_bf16_f32 v61, v174, v175\n"
"v_cvt_pk_bf16_f32 v62, v188, v189\n"
"v_cvt_pk_bf16_f32 v63, v190, v191\n"
"v_mfma_f32_16x16x32_bf16 v[72:75], a[184:187], v[56:59], v[72:75]\n"
"buffer_load_dwordx4 v246, s[44:47], s25 offen nt lds\n"
"v_mfma_f32_16x16x32_bf16 v[76:79], a[184:187], v[60:63], v[76:79]\n"
"s_waitcnt lgkmcnt(1)\n"
"s_add_u32 s25, s25, 4096\n"
"ds_read_b128 a[164:167], v241 offset:8192\n"
"v_mfma_f32_16x16x32_bf16 v[92:95], a[188:191], v[60:63], v[92:95]\n"
"v_mfma_f32_16x16x32_bf16 v[88:91], a[188:191], v[56:59], v[88:91]\n"
"s_add_u32 m0, s20, 45056\n"
"v_mfma_f32_16x16x32_bf16 v[84:87], a[188:191], v[52:55], v[84:87]\n"
"buffer_load_dwordx4 v246, s[44:47], s25 offen nt lds\n"
"v_mfma_f32_16x16x32_bf16 v[80:83], a[188:191], v[48:51], v[80:83]\n"
"s_waitcnt lgkmcnt(1)\n"
"s_add_u32 s25, s25, 4096\n"
"ds_read_b128 a[168:171], v240 offset:8704\n"
"v_max3_f32 v160, v64, v65, v66\n"
"v_max3_f32 v164, v68, v69, v70\n"
"v_max3_f32 v168, v72, v73, v74\n"
"v_max3_f32 v172, v76, v77, v78\n"
"v_max3_f32 v173, v79, v92, v93\n"
"v_max3_f32 v172, v172, v94, v95\n"
"v_max_f32 v172, v172, v173\n"
"v_max3_f32 v169, v75, v88, v89\n"
"v_max3_f32 v168, v168, v90, v91\n"
"v_max_f32 v168, v168, v169\n"
"v_max3_f32 v165, v71, v84, v85\n"
"v_max3_f32 v164, v164, v86, v87\n"
"v_max_f32 v164, v164, v165\n"
"v_max3_f32 v161, v67, v80, v81\n"
"v_max3_f32 v160, v160, v82, v83\n"
"v_max_f32 v160, v160, v161\n"
"v_mov_b32 v161, v160\n"
"v_mov_b32 v165, v164\n"
"v_mov_b32 v169, v168\n"
"v_mov_b32 v173, v172\n"
"v_permlane32_swap_b32 v160, v161\n"
"v_permlane32_swap_b32 v164, v165\n"
"v_permlane32_swap_b32 v168, v169\n"
"v_permlane32_swap_b32 v172, v173\n"
"v_max_f32 v160, v160, v161\n"
"v_mov_b32 v161, v160\n"
"v_max_f32 v164, v164, v165\n"
"v_mov_b32 v165, v164\n"
"v_max_f32 v168, v168, v169\n"
"v_mov_b32 v169, v168\n"
"v_max_f32 v172, v172, v173\n"
"v_mov_b32 v173, v172\n"
"v_permlane16_swap_b32 v160, v161\n"
"v_permlane16_swap_b32 v164, v165\n"
"v_permlane16_swap_b32 v168, v169\n"
"v_permlane16_swap_b32 v172, v173\n"
"v_min_f32 v160, -v160, -v161\n"
"v_min_f32 v164, -v164, -v165\n"
"v_min_f32 v168, -v168, -v169\n"
"v_min_f32 v172, -v172, -v173\n"
"v_mov_b32 v161, v160\n"
"v_mov_b32 v162, v160\n"
"v_mov_b32 v163, v160\n"
"v_mov_b32 v165, v164\n"
"v_mov_b32 v166, v164\n"
"v_mov_b32 v167, v164\n"
"v_mov_b32 v169, v168\n"
"v_mov_b32 v170, v168\n"
"v_mov_b32 v171, v168\n"
"v_mov_b32 v173, v172\n"
"v_mov_b32 v174, v172\n"
"v_mov_b32 v175, v172\n"
"v_add_f32 v64, v160, v64\n"
"v_add_f32 v65, v160, v65\n"
"v_add_f32 v66, v160, v66\n"
"v_add_f32 v67, v160, v67\n"
"v_add_f32 v68, v164, v68\n"
"v_add_f32 v69, v164, v69\n"
"v_add_f32 v70, v164, v70\n"
"v_add_f32 v71, v164, v71\n"
"v_add_f32 v72, v168, v72\n"
"v_add_f32 v73, v168, v73\n"
"v_add_f32 v74, v168, v74\n"
"v_add_f32 v75, v168, v75\n"
"v_add_f32 v76, v172, v76\n"
"v_add_f32 v77, v172, v77\n"
"v_add_f32 v78, v172, v78\n"
"v_add_f32 v79, v172, v79\n"
"v_add_f32 v80, v160, v80\n"
"v_add_f32 v81, v160, v81\n"
"v_add_f32 v82, v160, v82\n"
"v_add_f32 v83, v160, v83\n"
"v_add_f32 v84, v164, v84\n"
"v_add_f32 v85, v164, v85\n"
"v_add_f32 v86, v164, v86\n"
"v_add_f32 v87, v164, v87\n"
"v_add_f32 v88, v168, v88\n"
"v_add_f32 v89, v168, v89\n"
"v_add_f32 v90, v168, v90\n"
"v_add_f32 v91, v168, v91\n"
"v_add_f32 v92, v172, v92\n"
"v_add_f32 v93, v172, v93\n"
"v_add_f32 v94, v172, v94\n"
"v_add_f32 v95, v172, v95\n"
"v_exp_f32 v64, v64\n"
"v_exp_f32 v65, v65\n"
"v_exp_f32 v66, v66\n"
"v_exp_f32 v67, v67\n"
"v_exp_f32 v68, v68\n"
"v_exp_f32 v69, v69\n"
"v_exp_f32 v70, v70\n"
"v_exp_f32 v71, v71\n"
"v_exp_f32 v72, v72\n"
"v_exp_f32 v73, v73\n"
"v_exp_f32 v74, v74\n"
"v_exp_f32 v75, v75\n"
"v_exp_f32 v76, v76\n"
"v_exp_f32 v77, v77\n"
"v_exp_f32 v78, v78\n"
"v_exp_f32 v79, v79\n"
"v_cvt_pk_bf16_f32 v128, v64, v65\n"
"v_cvt_pk_bf16_f32 v129, v66, v67\n"
"v_cvt_pk_bf16_f32 v132, v68, v69\n"
"v_cvt_pk_bf16_f32 v133, v70, v71\n"
"v_cvt_pk_bf16_f32 v136, v72, v73\n"
"v_cvt_pk_bf16_f32 v137, v74, v75\n"
"v_cvt_pk_bf16_f32 v140, v76, v77\n"
"v_cvt_pk_bf16_f32 v141, v78, v79\n"
"v_mfma_f32_16x16x32_bf16 v[96:99], a[160:163], v[0:3], v[160:163]\n"
"v_exp_f32 v80, v80\n"
"v_mfma_f32_16x16x32_bf16 v[100:103], a[160:163], v[4:7], v[164:167]\n"
"v_mfma_f32_16x16x32_bf16 v[104:107], a[160:163], v[8:11], v[168:171]\n"
"v_exp_f32 v81, v81\n"
"v_mfma_f32_16x16x32_bf16 v[108:111], a[160:163], v[12:15], v[172:175]\n"
"s_waitcnt lgkmcnt(1)\n"
"ds_read_b128 a[172:175], v241 offset:8704\n"
"v_cvt_pk_bf16_f32 v130, v80, v81\n"
"v_mfma_f32_16x16x32_bf16 v[108:111], a[164:167], v[28:31], v[108:111]\n"
"s_add_u32 m0, s20, 57344\n"
"v_exp_f32 v82, v82\n"
"v_mfma_f32_16x16x32_bf16 v[104:107], a[164:167], v[24:27], v[104:107]\n"
"buffer_load_dwordx4 v247, s[48:51], s26 offen nt lds\n"
"v_mfma_f32_16x16x32_bf16 v[100:103], a[164:167], v[20:23], v[100:103]\n"
"s_add_u32 s26, s26, 4096\n"
"v_exp_f32 v83, v83\n"
"v_mfma_f32_16x16x32_bf16 v[96:99], a[164:167], v[16:19], v[96:99]\n"
"s_waitcnt lgkmcnt(1)\n"
"ds_read_b128 a[160:163], v240 offset:12288\n"
"v_cvt_pk_bf16_f32 v131, v82, v83\n"
"v_mfma_f32_16x16x32_bf16 v[96:99], a[168:171], v[32:35], v[96:99]\n"
"v_exp_f32 v84, v84\n"
"v_mfma_f32_16x16x32_bf16 v[100:103], a[168:171], v[36:39], v[100:103]\n"
"v_mfma_f32_16x16x32_bf16 v[104:107], a[168:171], v[40:43], v[104:107]\n"
"v_exp_f32 v85, v85\n"
"v_mfma_f32_16x16x32_bf16 v[108:111], a[168:171], v[44:47], v[108:111]\n"
"s_waitcnt lgkmcnt(1)\n"
"ds_read_b128 a[164:167], v241 offset:12288\n"
"v_cvt_pk_bf16_f32 v134, v84, v85\n"
"v_mfma_f32_16x16x32_bf16 v[108:111], a[172:175], v[60:63], v[108:111]\n"
"s_add_u32 m0, s20, 61440\n"
"v_exp_f32 v86, v86\n"
"v_mfma_f32_16x16x32_bf16 v[104:107], a[172:175], v[56:59], v[104:107]\n"
"buffer_load_dwordx4 v247, s[48:51], s26 offen nt lds\n"
"v_mfma_f32_16x16x32_bf16 v[100:103], a[172:175], v[52:55], v[100:103]\n"
"s_add_u32 s26, s26, 4096\n"
"v_exp_f32 v87, v87\n"
"v_mfma_f32_16x16x32_bf16 v[96:99], a[172:175], v[48:51], v[96:99]\n"
"s_waitcnt lgkmcnt(1)\n"
"ds_read_b128 a[168:171], v240 offset:12800\n"
"v_cvt_pk_bf16_f32 v135, v86, v87\n"
"v_mfma_f32_16x16x32_bf16 v[112:115], a[160:163], v[0:3], v[160:163]\n"
"v_exp_f32 v88, v88\n"
"v_mfma_f32_16x16x32_bf16 v[116:119], a[160:163], v[4:7], v[164:167]\n"
"v_mfma_f32_16x16x32_bf16 v[120:123], a[160:163], v[8:11], v[168:171]\n"
"v_exp_f32 v89, v89\n"
"v_mfma_f32_16x16x32_bf16 v[124:127], a[160:163], v[12:15], v[172:175]\n"
"s_waitcnt lgkmcnt(1)\n"
"ds_read_b128 a[172:175], v241 offset:12800\n"
"v_cvt_pk_bf16_f32 v138, v88, v89\n"
"v_mfma_f32_16x16x32_bf16 v[124:127], a[164:167], v[28:31], v[124:127]\n"
"v_exp_f32 v90, v90\n"
"v_mfma_f32_16x16x32_bf16 v[120:123], a[164:167], v[24:27], v[120:123]\n"
"ds_read_b64_tr_b16 a[160:161], v242 offset:8192\n"
"v_mfma_f32_16x16x32_bf16 v[116:119], a[164:167], v[20:23], v[116:119]\n"
"v_exp_f32 v91, v91\n"
"v_mfma_f32_16x16x32_bf16 v[112:115], a[164:167], v[16:19], v[112:115]\n"
"s_waitcnt lgkmcnt(2)\n"
"ds_read_b64_tr_b16 a[162:163], v242 offset:12288\n"
"v_cvt_pk_bf16_f32 v139, v90, v91\n"
"v_mfma_f32_16x16x32_bf16 v[112:115], a[168:171], v[32:35], v[112:115]\n"
"v_exp_f32 v92, v92\n"
"v_mfma_f32_16x16x32_bf16 v[116:119], a[168:171], v[36:39], v[116:119]\n"
"ds_read_b64_tr_b16 a[164:165], v243 offset:8192\n"
"v_add_u32 v240, s22, v240\n"
"v_mfma_f32_16x16x32_bf16 v[120:123], a[168:171], v[40:43], v[120:123]\n"
"v_exp_f32 v93, v93\n"
"v_mfma_f32_16x16x32_bf16 v[124:127], a[168:171], v[44:47], v[124:127]\n"
"s_waitcnt lgkmcnt(3)\n"
"ds_read_b64_tr_b16 a[166:167], v243 offset:12288\n"
"v_cvt_pk_bf16_f32 v142, v92, v93\n"
"v_mfma_f32_16x16x32_bf16 v[124:127], a[172:175], v[60:63], v[124:127]\n"
"v_exp_f32 v94, v94\n"
"v_mfma_f32_16x16x32_bf16 v[120:123], a[172:175], v[56:59], v[120:123]\n"
"ds_read_b64_tr_b16 a[168:169], v244 offset:8192\n"
"v_add_u32 v241, s22, v241\n"
"v_mfma_f32_16x16x32_bf16 v[116:119], a[172:175], v[52:55], v[116:119]\n"
"v_exp_f32 v95, v95\n"
"v_mfma_f32_16x16x32_bf16 v[112:115], a[172:175], v[48:51], v[112:115]\n"
"s_waitcnt lgkmcnt(3)\n"
"ds_read_b64_tr_b16 a[170:171], v244 offset:12288\n"
"v_cvt_pk_bf16_f32 v143, v94, v95\n"
"v_mfma_f32_16x16x32_bf16 a[0:3], a[160:163], v[128:131], 0\n"
"v_exp_f32 v96, v96\n"
"v_mfma_f32_16x16x32_bf16 a[4:7], a[160:163], v[132:135], 0\n"
"ds_read_b64_tr_b16 a[172:173], v245 offset:8192\n"
"v_mfma_f32_16x16x32_bf16 a[8:11], a[160:163], v[136:139], 0\n"
"v_exp_f32 v97, v97\n"
"v_mfma_f32_16x16x32_bf16 a[12:15], a[160:163], v[140:143], 0\n"
"s_waitcnt lgkmcnt(3)\n"
"ds_read_b64_tr_b16 a[174:175], v245 offset:12288\n"
"v_cvt_pk_bf16_f32 v144, v96, v97\n"
"v_mfma_f32_16x16x32_bf16 a[28:31], a[164:167], v[140:143], 0\n"
"v_exp_f32 v98, v98\n"
"v_mfma_f32_16x16x32_bf16 a[24:27], a[164:167], v[136:139], 0\n"
"ds_read_b64_tr_b16 a[160:161], v242 offset:8704\n"
"v_mfma_f32_16x16x32_bf16 a[20:23], a[164:167], v[132:135], 0\n"
"v_exp_f32 v99, v99\n"
"v_mfma_f32_16x16x32_bf16 a[16:19], a[164:167], v[128:131], 0\n"
"s_waitcnt lgkmcnt(3)\n"
"ds_read_b64_tr_b16 a[162:163], v242 offset:12800\n"
"v_cvt_pk_bf16_f32 v145, v98, v99\n"
"v_mfma_f32_16x16x32_bf16 a[32:35], a[168:171], v[128:131], 0\n"
"v_exp_f32 v100, v100\n"
"v_mfma_f32_16x16x32_bf16 a[36:39], a[168:171], v[132:135], 0\n"
"ds_read_b64_tr_b16 a[164:165], v243 offset:8704\n"
"v_add_u32 v242, s22, v242\n"
"v_mfma_f32_16x16x32_bf16 a[40:43], a[168:171], v[136:139], 0\n"
"s_and_b32 s78, s36, 15\n"
"v_exp_f32 v101, v101\n"
"v_mfma_f32_16x16x32_bf16 a[44:47], a[168:171], v[140:143], 0\n"
"s_waitcnt lgkmcnt(3)\n"
"ds_read_b64_tr_b16 a[166:167], v243 offset:12800\n"
"v_cvt_pk_bf16_f32 v148, v100, v101\n"
"v_mfma_f32_16x16x32_bf16 a[60:63], a[172:175], v[140:143], 0\n"
"s_lshr_b64 s[36:37], s[36:37], 4\n"
"v_exp_f32 v102, v102\n"
"v_mfma_f32_16x16x32_bf16 a[56:59], a[172:175], v[136:139], 0\n"
"ds_read_b64_tr_b16 a[168:169], v244 offset:8704\n"
"v_add_u32 v243, s22, v243\n"
"v_mfma_f32_16x16x32_bf16 a[52:55], a[172:175], v[132:135], 0\n"
"s_lshl1_add_u32 s18, s78, s18\n"
"v_exp_f32 v103, v103\n"
"v_mfma_f32_16x16x32_bf16 a[48:51], a[172:175], v[128:131], 0\n"
"s_waitcnt lgkmcnt(3)\n"
"ds_read_b64_tr_b16 a[170:171], v244 offset:12800\n"
"v_cvt_pk_bf16_f32 v149, v102, v103\n"
"v_mfma_f32_16x16x32_bf16 a[64:67], a[160:163], v[128:131], 0\n"
"v_exp_f32 v104, v104\n"
"v_mfma_f32_16x16x32_bf16 a[68:71], a[160:163], v[132:135], 0\n"
"ds_read_b64_tr_b16 a[172:173], v245 offset:8704\n"
"v_add_u32 v244, s22, v244\n"
"v_mfma_f32_16x16x32_bf16 a[72:75], a[160:163], v[136:139], 0\n"
"s_add_u32 s23, s24, s20\n"
"v_exp_f32 v105, v105\n"
"v_mfma_f32_16x16x32_bf16 a[76:79], a[160:163], v[140:143], 0\n"
"s_waitcnt lgkmcnt(3)\n"
"ds_read_b64_tr_b16 a[174:175], v245 offset:12800\n"
"v_cvt_pk_bf16_f32 v152, v104, v105\n"
"v_mfma_f32_16x16x32_bf16 a[92:95], a[164:167], v[140:143], 0\n"
"s_memrealtime s[82:83]\n"
"v_exp_f32 v106, v106\n"
"v_mfma_f32_16x16x32_bf16 a[88:91], a[164:167], v[136:139], 0\n"
"v_add_u32 v245, s22, v245\n"
"v_mfma_f32_16x16x32_bf16 a[84:87], a[164:167], v[132:135], 0\n"
"s_add_u32 s18, s18, 131\n"
"v_exp_f32 v107, v107\n"
"v_mfma_f32_16x16x32_bf16 a[80:83], a[164:167], v[128:131], 0\n"
"s_waitcnt lgkmcnt(2)\n"
"v_cvt_pk_bf16_f32 v153, v106, v107\n"
"v_mfma_f32_16x16x32_bf16 a[96:99], a[168:171], v[128:131], a[96:99]\n"
"s_add_u32 s24, s24, s22\n"
"v_exp_f32 v108, v108\n"
"v_mfma_f32_16x16x32_bf16 a[100:103], a[168:171], v[132:135], a[100:103]\n"
"s_waitcnt vmcnt(8) lgkmcnt(0)\n"
"s_barrier\n"
"v_mfma_f32_16x16x32_bf16 a[104:107], a[168:171], v[136:139], a[104:107]\n"
"s_sub_u32 s83, s82, s19\n"
"v_exp_f32 v109, v109\n"
"v_mfma_f32_16x16x32_bf16 a[108:111], a[168:171], v[140:143], a[108:111]\n"
"s_cmp_lt_i32 s83, 130\n"
"ds_read_b128 a[160:163], v240 offset:0\n"
"v_cvt_pk_bf16_f32 v156, v108, v109\n"
"v_mfma_f32_16x16x32_bf16 a[124:127], a[172:175], v[140:143], a[124:127]\n"
"s_cbranch_scc1 L_pacea0_%=\n"
"L_paceba0_%=:\n"
"v_exp_f32 v110, v110\n"
"v_mfma_f32_16x16x32_bf16 a[120:123], a[172:175], v[136:139], a[120:123]\n"
"s_sub_u32 s83, s82, s18\n"
"s_max_i32 s83, s83, 0\n"
"s_add_u32 s19, s18, s83\n"
"v_mfma_f32_16x16x32_bf16 a[116:119], a[172:175], v[132:135], a[116:119]\n"
"v_exp_f32 v111, v111\n"
"v_mfma_f32_16x16x32_bf16 a[112:115], a[172:175], v[128:131], a[112:115]\n"
"ds_read_b128 a[164:167], v241 offset:0\n"
"v_cvt_pk_bf16_f32 v157, v110, v111\n"
"v_smfmac_f32_16x16x64_bf16 a[128:131], a[144:147], v[128:135], v239\n"
"buffer_store_dwordx4 a[148:151], v208, s[60:63], s75 offen sc1\n"
"s_mov_b32 s21, 12\n"
"s_branch L_body_%=\n"
"L_pacea0_%=:\n"
"s_sleep 1\n"
"s_memrealtime s[82:83]\n"
"s_waitcnt lgkmcnt(0)\n"
"s_sub_u32 s83, s82, s19\n"
"s_cmp_lt_i32 s83, 134\n"
"s_cbranch_scc1 L_pacea0_%=\n"
"s_add_u32 s82, s19, 130\n"
"s_branch L_paceba0_%=\n"
"L_pacea1_%=:\n"
"s_sleep 1\n"
"s_memrealtime s[82:83]\n"
"s_waitcnt lgkmcnt(0)\n"
"s_sub_u32 s83, s82, s19\n"
"s_cmp_lt_i32 s83, 134\n"
"s_cbranch_scc1 L_pacea1_%=\n"
"s_add_u32 s82, s19, 130\n"
"s_branch L_paceba1_%=\n"
".p2align 3\n"
"L_body_%=:\n"
"v_smfmac_f32_16x16x64_bf16 a[132:135], a[144:147], v[136:143], v239\n"
"s_waitcnt lgkmcnt(1)\n"
"ds_read_b128 a[168:171], v240 offset:512\n"
"v_mfma_f32_16x16x32_bf16 v[64:67], a[160:163], v[0:3], v[160:163]\n"
"s_add_u32 m0, s23, 0\n"
"v_exp_f32 v112, v112\n"
"v_mfma_f32_16x16x32_bf16 v[68:71], a[160:163], v[4:7], v[164:167]\n"
"buffer_load_dwordx4 v246, s[44:47], s25 offen nt lds\n"
"v_mfma_f32_16x16x32_bf16 v[72:75], a[160:163], v[8:11], v[168:171]\n"
"s_add_u32 s25, s25, 4096\n"
"v_exp_f32 v113, v113\n"
"v_mfma_f32_16x16x32_bf16 v[76:79], a[160:163], v[12:15], v[172:175]\n"
"s_waitcnt lgkmcnt(1)\n"
"ds_read_b128 a[172:175], v241 offset:512\n"
"v_cvt_pk_bf16_f32 v146, v112, v113\n"
"v_mfma_f32_16x16x32_bf16 v[76:79], a[164:167], v[28:31], v[76:79]\n"
"s_add_u32 m0, s23, 4096\n"
"v_exp_f32 v114, v114\n"
"v_mfma_f32_16x16x32_bf16 v[72:75], a[164:167], v[24:27], v[72:75]\n"
"buffer_load_dwordx4 v246, s[44:47], s25 offen nt lds\n"
"v_mfma_f32_16x16x32_bf16 v[68:71], a[164:167], v[20:23], v[68:71]\n"
"s_add_u32 s25, s25, 4096\n"
"v_exp_f32 v115, v115\n"
"v_mfma_f32_16x16x32_bf16 v[64:67], a[164:167], v[16:19], v[64:67]\n"
"s_waitcnt lgkmcnt(1)\n"
"ds_read_b128 a[160:163], v240 offset:4096\n"
"v_cvt_pk_bf16_f32 v147, v114, v115\n"
"v_mfma_f32_16x16x32_bf16 v[64:67], a[168:171], v[32:35], v[64:67]\n"
"s_add_u32 m0, s23, 16384\n"
"v_exp_f32 v116, v116\n"
"v_mfma_f32_16x16x32_bf16 v[68:71], a[168:171], v[36:39], v[68:71]\n"
"buffer_load_dwordx4 v247, s[48:51], s26 offen nt lds\n"
"v_mfma_f32_16x16x32_bf16 v[72:75], a[168:171], v[40:43], v[72:75]\n"
"s_add_u32 s26, s26, 4096\n"
"v_exp_f32 v117, v117\n"
"v_mfma_f32_16x16x32_bf16 v[76:79], a[168:171], v[44:47], v[76:79]\n"
"s_waitcnt lgkmcnt(1)\n"
"ds_read_b128 a[164:167], v241 offset:4096\n"
"v_cvt_pk_bf16_f32 v150, v116, v117\n"
"v_mfma_f32_16x16x32_bf16 v[76:79], a[172:175], v[60:63], v[76:79]\n"
"s_add_u32 m0, s23, 20480\n"
"v_exp_f32 v118, v118\n"
"v_mfma_f32_16x16x32_bf16 v[72:75], a[172:175], v[56:59], v[72:75]\n"
"buffer_load_dwordx4 v247, s[48:51], s26 offen nt lds\n"
"v_mfma_f32_16x16x32_bf16 v[68:71], a[172:175], v[52:55], v[68:71]\n"
"s_add_u32 s26, s26, 4096\n"
"v_exp_f32 v119, v119\n"
"v_mfma_f32_16x16x32_bf16 v[64:67], a[172:175], v[48:51], v[64:67]\n"
"s_waitcnt lgkmcnt(1)\n"
"ds_read_b128 a[168:171], v240 offset:4608\n"
"v_cvt_pk_bf16_f32 v151, v118, v119\n"
"v_mfma_f32_16x16x32_bf16 v[80:83], a[160:163], v[0:3], v[160:163]\n"
"v_exp_f32 v120, v120\n"
"v_mfma_f32_16x16x32_bf16 v[84:87], a[160:163], v[4:7], v[164:167]\n"
"global_load_dword a157, v[220:221], off sc1\n"
"v_mfma_f32_16x16x32_bf16 v[88:91], a[160:163], v[8:11], v[168:171]\n"
"v_exp_f32 v121, v121\n"
"v_mfma_f32_16x16x32_bf16 v[92:95], a[160:163], v[12:15], v[172:175]\n"
"s_waitcnt lgkmcnt(1)\n"
"ds_read_b128 a[172:175], v241 offset:4608\n"
"v_cvt_pk_bf16_f32 v154, v120, v121\n"
"v_mfma_f32_16x16x32_bf16 v[92:95], a[164:167], v[28:31], v[92:95]\n"
"s_cmp_eq_u32 s24, 0\n"
"v_exp_f32 v122, v122\n"
"v_mfma_f32_16x16x32_bf16 v[88:91], a[164:167], v[24:27], v[88:91]\n"
"s_cselect_b32 s22, 2, -1\n"
"ds_read_b64_tr_b16 a[160:161], v242 offset:0\n"
"v_mfma_f32_16x16x32_bf16 v[84:87], a[164:167], v[20:23], v[84:87]\n"
"s_lshl_b32 s22, s22, 15\n"
"v_exp_f32 v123, v123\n"
"v_mfma_f32_16x16x32_bf16 v[80:83], a[164:167], v[16:19], v[80:83]\n"
"s_waitcnt lgkmcnt(2)\n"
"ds_read_b64_tr_b16 a[162:163], v242 offset:4096\n"
"v_cvt_pk_bf16_f32 v155, v122, v123\n"
"v_mfma_f32_16x16x32_bf16 v[80:83], a[168:171], v[32:35], v[80:83]\n"
"v_exp_f32 v124, v124\n"
"v_mfma_f32_16x16x32_bf16 v[84:87], a[168:171], v[36:39], v[84:87]\n"
"ds_read_b64_tr_b16 a[164:165], v243 offset:0\n"
"v_mfma_f32_16x16x32_bf16 v[88:91], a[168:171], v[40:43], v[88:91]\n"
"v_exp_f32 v125, v125\n"
"v_mfma_f32_16x16x32_bf16 v[92:95], a[168:171], v[44:47], v[92:95]\n"
"s_waitcnt lgkmcnt(3)\n"
"ds_read_b64_tr_b16 a[166:167], v243 offset:4096\n"
"v_cvt_pk_bf16_f32 v158, v124, v125\n"
"v_mfma_f32_16x16x32_bf16 v[92:95], a[172:175], v[60:63], v[92:95]\n"
"v_exp_f32 v126, v126\n"
"v_mfma_f32_16x16x32_bf16 v[88:91], a[172:175], v[56:59], v[88:91]\n"
"ds_read_b64_tr_b16 a[168:169], v244 offset:0\n"
"v_mfma_f32_16x16x32_bf16 v[84:87], a[172:175], v[52:55], v[84:87]\n"
"v_exp_f32 v127, v127\n"
"v_mfma_f32_16x16x32_bf16 v[80:83], a[172:175], v[48:51], v[80:83]\n"
"s_waitcnt lgkmcnt(3)\n"
"ds_read_b64_tr_b16 a[170:171], v244 offset:4096\n"
"v_cvt_pk_bf16_f32 v159, v126, v127\n"
"v_mfma_f32_16x16x32_bf16 a[0:3], a[160:163], v[144:147], a[0:3]\n"
"v_exp_f32 v64, v64\n"
"v_mfma_f32_16x16x32_bf16 a[4:7], a[160:163], v[148:151], a[4:7]\n"
"ds_read_b64_tr_b16 a[172:173], v245 offset:0\n"
"v_lshl_add_u64 v[220:221], v[222:223], 0, v[220:221]\n"
"v_mfma_f32_16x16x32_bf16 a[8:11], a[160:163], v[152:155], a[8:11]\n"
"v_exp_f32 v65, v65\n"
"v_mfma_f32_16x16x32_bf16 a[12:15], a[160:163], v[156:159], a[12:15]\n"
"s_waitcnt lgkmcnt(3)\n"
"ds_read_b64_tr_b16 a[174:175], v245 offset:4096\n"
"v_cvt_pk_bf16_f32 v128, v64, v65\n"
"v_mfma_f32_16x16x32_bf16 a[28:31], a[164:167], v[156:159], a[28:31]\n"
"s_lshl_b32 s72, s70, 3\n"
"v_exp_f32 v66, v66\n"
"v_mfma_f32_16x16x32_bf16 a[24:27], a[164:167], v[152:155], a[24:27]\n"
"ds_read_b64_tr_b16 a[160:161], v242 offset:512\n"
"v_mfma_f32_16x16x32_bf16 a[20:23], a[164:167], v[148:151], a[20:23]\n"
"s_add_u32 s72, s72, s80\n"
"v_exp_f32 v67, v67\n"
"v_mfma_f32_16x16x32_bf16 a[16:19], a[164:167], v[144:147], a[16:19]\n"
"s_waitcnt lgkmcnt(3)\n"
"ds_read_b64_tr_b16 a[162:163], v242 offset:4608\n"
"v_cvt_pk_bf16_f32 v129, v66, v67\n"
"v_mfma_f32_16x16x32_bf16 a[32:35], a[168:171], v[144:147], a[32:35]\n"
"s_and_b32 s68, s70, 12288\n"
"v_exp_f32 v68, v68\n"
"v_mfma_f32_16x16x32_bf16 a[36:39], a[168:171], v[148:151], a[36:39]\n"
"ds_read_b64_tr_b16 a[164:165], v243 offset:512\n"
"v_mfma_f32_16x16x32_bf16 a[40:43], a[168:171], v[152:155], a[40:43]\n"
"s_mul_i32 s68, s68, 3\n"
"v_exp_f32 v69, v69\n"
"v_mfma_f32_16x16x32_bf16 a[44:47], a[168:171], v[156:159], a[44:47]\n"
"s_waitcnt lgkmcnt(3)\n"
"ds_read_b64_tr_b16 a[166:167], v243 offset:4608\n"
"v_cvt_pk_bf16_f32 v132, v68, v69\n"
"v_mfma_f32_16x16x32_bf16 a[60:63], a[172:175], v[156:159], a[60:63]\n"
"s_add_u32 s69, s70, s68\n"
"v_exp_f32 v70, v70\n"
"v_mfma_f32_16x16x32_bf16 a[56:59], a[172:175], v[152:155], a[56:59]\n"
"ds_read_b64_tr_b16 a[168:169], v244 offset:512\n"
"v_mfma_f32_16x16x32_bf16 a[52:55], a[172:175], v[148:151], a[52:55]\n"
"s_add_u32 s69, s69, s33\n"
"v_exp_f32 v71, v71\n"
"v_mfma_f32_16x16x32_bf16 a[48:51], a[172:175], v[144:147], a[48:51]\n"
"s_waitcnt lgkmcnt(3)\n"
"ds_read_b64_tr_b16 a[170:171], v244 offset:4608\n"
"v_cvt_pk_bf16_f32 v133, v70, v71\n"
"v_mfma_f32_16x16x32_bf16 a[64:67], a[160:163], v[144:147], a[64:67]\n"
"s_add_u32 s70, s70, 1024\n"
"v_exp_f32 v72, v72\n"
"v_mfma_f32_16x16x32_bf16 a[68:71], a[160:163], v[148:151], a[68:71]\n"
"ds_read_b64_tr_b16 a[172:173], v245 offset:512\n"
"v_mfma_f32_16x16x32_bf16 a[72:75], a[160:163], v[152:155], a[72:75]\n"
"v_exp_f32 v73, v73\n"
"v_mfma_f32_16x16x32_bf16 a[76:79], a[160:163], v[156:159], a[76:79]\n"
"s_waitcnt lgkmcnt(3)\n"
"ds_read_b64_tr_b16 a[174:175], v245 offset:4608\n"
"v_cvt_pk_bf16_f32 v136, v72, v73\n"
"v_mfma_f32_16x16x32_bf16 a[92:95], a[164:167], v[156:159], a[92:95]\n"
"v_exp_f32 v74, v74\n"
"v_mfma_f32_16x16x32_bf16 a[88:91], a[164:167], v[152:155], a[88:91]\n"
"v_mfma_f32_16x16x32_bf16 a[84:87], a[164:167], v[148:151], a[84:87]\n"
"v_exp_f32 v75, v75\n"
"v_mfma_f32_16x16x32_bf16 a[80:83], a[164:167], v[144:147], a[80:83]\n"
"s_waitcnt lgkmcnt(2)\n"
"v_cvt_pk_bf16_f32 v137, v74, v75\n"
"v_mfma_f32_16x16x32_bf16 a[96:99], a[168:171], v[144:147], a[96:99]\n"
"s_add_u32 m0, s23, 8192\n"
"v_exp_f32 v76, v76\n"
"v_mfma_f32_16x16x32_bf16 a[100:103], a[168:171], v[148:151], a[100:103]\n"
"buffer_load_dwordx4 v246, s[44:47], s25 offen nt lds\n"
"v_mfma_f32_16x16x32_bf16 a[104:107], a[168:171], v[152:155], a[104:107]\n"
"s_add_u32 s25, s25, 4096\n"
"v_exp_f32 v77, v77\n"
"v_mfma_f32_16x16x32_bf16 a[108:111], a[168:171], v[156:159], a[108:111]\n"
"s_waitcnt lgkmcnt(0)\n"
"ds_read_b128 a[160:163], v240 offset:8192\n"
"v_cvt_pk_bf16_f32 v140, v76, v77\n"
"v_mfma_f32_16x16x32_bf16 a[124:127], a[172:175], v[156:159], a[124:127]\n"
"s_add_u32 m0, s23, 12288\n"
"v_exp_f32 v78, v78\n"
"v_mfma_f32_16x16x32_bf16 a[120:123], a[172:175], v[152:155], a[120:123]\n"
"buffer_load_dwordx4 v246, s[44:47], s25 offen nt lds\n"
"v_mfma_f32_16x16x32_bf16 a[116:119], a[172:175], v[148:151], a[116:119]\n"
"s_add_u32 s25, s25, 4096\n"
"v_exp_f32 v79, v79\n"
"v_mfma_f32_16x16x32_bf16 a[112:115], a[172:175], v[144:147], a[112:115]\n"
"s_add_u32 m0, s23, 24576\n"
"ds_read_b128 a[164:167], v241 offset:8192\n"
"v_cvt_pk_bf16_f32 v141, v78, v79\n"
"v_smfmac_f32_16x16x64_bf16 a[128:131], a[144:147], v[144:151], v239\n"
"buffer_load_dwordx4 v247, s[48:51], s26 offen nt lds\n"
"v_smfmac_f32_16x16x64_bf16 a[132:135], a[144:147], v[152:159], v239\n"
"s_waitcnt lgkmcnt(1)\n"
"s_add_u32 s26, s26, 4096\n"
"ds_read_b128 a[168:171], v240 offset:8704\n"
"v_mfma_f32_16x16x32_bf16 v[96:99], a[160:163], v[0:3], v[160:163]\n"
"s_add_u32 m0, s23, 28672\n"
"v_exp_f32 v80, v80\n"
"v_mfma_f32_16x16x32_bf16 v[100:103], a[160:163], v[4:7], v[164:167]\n"
"buffer_load_dwordx4 v247, s[48:51], s26 offen nt lds\n"
"v_mfma_f32_16x16x32_bf16 v[104:107], a[160:163], v[8:11], v[168:171]\n"
"s_add_u32 s26, s26, 4096\n"
"v_exp_f32 v81, v81\n"
"v_mfma_f32_16x16x32_bf16 v[108:111], a[160:163], v[12:15], v[172:175]\n"
"s_waitcnt lgkmcnt(1)\n"
"ds_read_b128 a[172:175], v241 offset:8704\n"
"v_cvt_pk_bf16_f32 v130, v80, v81\n"
"v_mfma_f32_16x16x32_bf16 v[108:111], a[164:167], v[28:31], v[108:111]\n"
"s_add_u32 m0, s69, 98304\n"
"v_exp_f32 v82, v82\n"
"v_mfma_f32_16x16x32_bf16 v[104:107], a[164:167], v[24:27], v[104:107]\n"
"buffer_load_dwordx4 v248, s[56:59], s72 offen lds\n"
"v_mfma_f32_16x16x32_bf16 v[100:103], a[164:167], v[20:23], v[100:103]\n"
"v_exp_f32 v83, v83\n"
"v_mfma_f32_16x16x32_bf16 v[96:99], a[164:167], v[16:19], v[96:99]\n"
"s_waitcnt lgkmcnt(1)\n"
"ds_read_b128 a[160:163], v240 offset:12288\n"
"v_cvt_pk_bf16_f32 v131, v82, v83\n"
"v_mfma_f32_16x16x32_bf16 v[96:99], a[168:171], v[32:35], v[96:99]\n"
"v_exp_f32 v84, v84\n"
"v_mfma_f32_16x16x32_bf16 v[100:103], a[168:171], v[36:39], v[100:103]\n"
"v_mfma_f32_16x16x32_bf16 v[104:107], a[168:171], v[40:43], v[104:107]\n"
"v_exp_f32 v85, v85\n"
"v_mfma_f32_16x16x32_bf16 v[108:111], a[168:171], v[44:47], v[108:111]\n"
"s_waitcnt lgkmcnt(1)\n"
"ds_read_b128 a[164:167], v241 offset:12288\n"
"v_cvt_pk_bf16_f32 v134, v84, v85\n"
"v_mfma_f32_16x16x32_bf16 v[108:111], a[172:175], v[60:63], v[108:111]\n"
"v_exp_f32 v86, v86\n"
"v_mfma_f32_16x16x32_bf16 v[104:107], a[172:175], v[56:59], v[104:107]\n"
"v_mfma_f32_16x16x32_bf16 v[100:103], a[172:175], v[52:55], v[100:103]\n"
"v_exp_f32 v87, v87\n"
"v_mfma_f32_16x16x32_bf16 v[96:99], a[172:175], v[48:51], v[96:99]\n"
"s_waitcnt lgkmcnt(1)\n"
"ds_read_b128 a[168:171], v240 offset:12800\n"
"v_cvt_pk_bf16_f32 v135, v86, v87\n"
"v_mfma_f32_16x16x32_bf16 v[112:115], a[160:163], v[0:3], v[160:163]\n"
"v_exp_f32 v88, v88\n"
"v_mfma_f32_16x16x32_bf16 v[116:119], a[160:163], v[4:7], v[164:167]\n"
"v_mfma_f32_16x16x32_bf16 v[120:123], a[160:163], v[8:11], v[168:171]\n"
"v_exp_f32 v89, v89\n"
"v_mfma_f32_16x16x32_bf16 v[124:127], a[160:163], v[12:15], v[172:175]\n"
"s_waitcnt lgkmcnt(1)\n"
"ds_read_b128 a[172:175], v241 offset:12800\n"
"v_cvt_pk_bf16_f32 v138, v88, v89\n"
"v_mfma_f32_16x16x32_bf16 v[124:127], a[164:167], v[28:31], v[124:127]\n"
"v_exp_f32 v90, v90\n"
"v_mfma_f32_16x16x32_bf16 v[120:123], a[164:167], v[24:27], v[120:123]\n"
"ds_read_b64_tr_b16 a[160:161], v242 offset:8192\n"
"v_mfma_f32_16x16x32_bf16 v[116:119], a[164:167], v[20:23], v[116:119]\n"
"v_exp_f32 v91, v91\n"
"v_mfma_f32_16x16x32_bf16 v[112:115], a[164:167], v[16:19], v[112:115]\n"
"s_waitcnt lgkmcnt(2)\n"
"ds_read_b64_tr_b16 a[162:163], v242 offset:12288\n"
"v_cvt_pk_bf16_f32 v139, v90, v91\n"
"v_mfma_f32_16x16x32_bf16 v[112:115], a[168:171], v[32:35], v[112:115]\n"
"v_exp_f32 v92, v92\n"
"v_mfma_f32_16x16x32_bf16 v[116:119], a[168:171], v[36:39], v[116:119]\n"
"ds_read_b64_tr_b16 a[164:165], v243 offset:8192\n"
"v_add_u32 v240, s22, v240\n"
"v_mfma_f32_16x16x32_bf16 v[120:123], a[168:171], v[40:43], v[120:123]\n"
"v_exp_f32 v93, v93\n"
"v_mfma_f32_16x16x32_bf16 v[124:127], a[168:171], v[44:47], v[124:127]\n"
"s_waitcnt lgkmcnt(3)\n"
"ds_read_b64_tr_b16 a[166:167], v243 offset:12288\n"
"v_cvt_pk_bf16_f32 v142, v92, v93\n"
"v_mfma_f32_16x16x32_bf16 v[124:127], a[172:175], v[60:63], v[124:127]\n"
"v_exp_f32 v94, v94\n"
"v_mfma_f32_16x16x32_bf16 v[120:123], a[172:175], v[56:59], v[120:123]\n"
"ds_read_b64_tr_b16 a[168:169], v244 offset:8192\n"
"v_add_u32 v241, s22, v241\n"
"v_mfma_f32_16x16x32_bf16 v[116:119], a[172:175], v[52:55], v[116:119]\n"
"v_exp_f32 v95, v95\n"
"v_mfma_f32_16x16x32_bf16 v[112:115], a[172:175], v[48:51], v[112:115]\n"
"s_waitcnt lgkmcnt(3)\n"
"ds_read_b64_tr_b16 a[170:171], v244 offset:12288\n"
"v_cvt_pk_bf16_f32 v143, v94, v95\n"
"v_mfma_f32_16x16x32_bf16 a[0:3], a[160:163], v[128:131], a[0:3]\n"
"v_exp_f32 v96, v96\n"
"v_mfma_f32_16x16x32_bf16 a[4:7], a[160:163], v[132:135], a[4:7]\n"
"ds_read_b64_tr_b16 a[172:173], v245 offset:8192\n"
"v_mfma_f32_16x16x32_bf16 a[8:11], a[160:163], v[136:139], a[8:11]\n"
"v_exp_f32 v97, v97\n"
"v_mfma_f32_16x16x32_bf16 a[12:15], a[160:163], v[140:143], a[12:15]\n"
"s_waitcnt lgkmcnt(3)\n"
"ds_read_b64_tr_b16 a[174:175], v245 offset:12288\n"
"v_cvt_pk_bf16_f32 v144, v96, v97\n"
"v_mfma_f32_16x16x32_bf16 a[28:31], a[164:167], v[140:143], a[28:31]\n"
"v_exp_f32 v98, v98\n"
"v_mfma_f32_16x16x32_bf16 a[24:27], a[164:167], v[136:139], a[24:27]\n"
"ds_read_b64_tr_b16 a[160:161], v242 offset:8704\n"
"v_mfma_f32_16x16x32_bf16 a[20:23], a[164:167], v[132:135], a[20:23]\n"
"v_exp_f32 v99, v99\n"
"v_mfma_f32_16x16x32_bf16 a[16:19], a[164:167], v[128:131], a[16:19]\n"
"s_waitcnt lgkmcnt(3)\n"
"ds_read_b64_tr_b16 a[162:163], v242 offset:12800\n"
"v_cvt_pk_bf16_f32 v145, v98, v99\n"
"v_mfma_f32_16x16x32_bf16 a[32:35], a[168:171], v[128:131], a[32:35]\n"
"v_exp_f32 v100, v100\n"
"v_mfma_f32_16x16x32_bf16 a[36:39], a[168:171], v[132:135], a[36:39]\n"
"ds_read_b64_tr_b16 a[164:165], v243 offset:8704\n"
"v_add_u32 v242, s22, v242\n"
"v_mfma_f32_16x16x32_bf16 a[40:43], a[168:171], v[136:139], a[40:43]\n"
"s_and_b32 s78, s36, 15\n"
"v_exp_f32 v101, v101\n"
"v_mfma_f32_16x16x32_bf16 a[44:47], a[168:171], v[140:143], a[44:47]\n"
"s_waitcnt lgkmcnt(3)\n"
"ds_read_b64_tr_b16 a[166:167], v243 offset:12800\n"
"v_cvt_pk_bf16_f32 v148, v100, v101\n"
"v_mfma_f32_16x16x32_bf16 a[60:63], a[172:175], v[140:143], a[60:63]\n"
"s_lshr_b64 s[36:37], s[36:37], 4\n"
"v_exp_f32 v102, v102\n"
"v_mfma_f32_16x16x32_bf16 a[56:59], a[172:175], v[136:139], a[56:59]\n"
"ds_read_b64_tr_b16 a[168:169], v244 offset:8704\n"
"v_add_u32 v243, s22, v243\n"
"v_mfma_f32_16x16x32_bf16 a[52:55], a[172:175], v[132:135], a[52:55]\n"
"s_lshl1_add_u32 s18, s78, s18\n"
"v_exp_f32 v103, v103\n"
"v_mfma_f32_16x16x32_bf16 a[48:51], a[172:175], v[128:131], a[48:51]\n"
"s_waitcnt lgkmcnt(3)\n"
"ds_read_b64_tr_b16 a[170:171], v244 offset:12800\n"
"v_cvt_pk_bf16_f32 v149, v102, v103\n"
"v_mfma_f32_16x16x32_bf16 a[64:67], a[160:163], v[128:131], a[64:67]\n"
"v_exp_f32 v104, v104\n"
"v_mfma_f32_16x16x32_bf16 a[68:71], a[160:163], v[132:135], a[68:71]\n"
"ds_read_b64_tr_b16 a[172:173], v245 offset:8704\n"
"v_add_u32 v244, s22, v244\n"
"v_mfma_f32_16x16x32_bf16 a[72:75], a[160:163], v[136:139], a[72:75]\n"
"s_add_u32 s23, s24, s20\n"
"v_exp_f32 v105, v105\n"
"v_mfma_f32_16x16x32_bf16 a[76:79], a[160:163], v[140:143], a[76:79]\n"
"s_waitcnt lgkmcnt(3)\n"
"ds_read_b64_tr_b16 a[174:175], v245 offset:12800\n"
"v_cvt_pk_bf16_f32 v152, v104, v105\n"
"v_mfma_f32_16x16x32_bf16 a[92:95], a[164:167], v[140:143], a[92:95]\n"
"s_memrealtime s[82:83]\n"
"v_exp_f32 v106, v106\n"
"v_mfma_f32_16x16x32_bf16 a[88:91], a[164:167], v[136:139], a[88:91]\n"
"v_add_u32 v245, s22, v245\n"
"v_mfma_f32_16x16x32_bf16 a[84:87], a[164:167], v[132:135], a[84:87]\n"
"s_add_u32 s18, s18, 131\n"
"v_exp_f32 v107, v107\n"
"v_mfma_f32_16x16x32_bf16 a[80:83], a[164:167], v[128:131], a[80:83]\n"
"s_waitcnt lgkmcnt(2)\n"
"v_cvt_pk_bf16_f32 v153, v106, v107\n"
"v_mfma_f32_16x16x32_bf16 a[96:99], a[168:171], v[128:131], a[96:99]\n"
"s_add_u32 s24, s24, s22\n"
"v_exp_f32 v108, v108\n"
"v_mfma_f32_16x16x32_bf16 a[100:103], a[168:171], v[132:135], a[100:103]\n"
"s_waitcnt vmcnt(11) lgkmcnt(0)\n"
"s_barrier\n"
"v_mfma_f32_16x16x32_bf16 a[104:107], a[168:171], v[136:139], a[104:107]\n"
"s_sub_u32 s83, s82, s19\n"
"v_exp_f32 v109, v109\n"
"v_mfma_f32_16x16x32_bf16 a[108:111], a[168:171], v[140:143], a[108:111]\n"
"s_cmp_lt_i32 s83, 130\n"
"ds_read_b128 a[160:163], v240 offset:0\n"
"v_cvt_pk_bf16_f32 v156, v108, v109\n"
"v_mfma_f32_16x16x32_bf16 a[124:127], a[172:175], v[140:143], a[124:127]\n"
"s_cbranch_scc1 L_pacea1_%=\n"
"L_paceba1_%=:\n"
"v_exp_f32 v110, v110\n"
"v_mfma_f32_16x16x32_bf16 a[120:123], a[172:175], v[136:139], a[120:123]\n"
"s_sub_u32 s83, s82, s18\n"
"s_max_i32 s83, s83, 0\n"
"s_add_u32 s19, s18, s83\n"
"v_mfma_f32_16x16x32_bf16 a[116:119], a[172:175], v[132:135], a[116:119]\n"
"v_exp_f32 v111, v111\n"
"v_mfma_f32_16x16x32_bf16 a[112:115], a[172:175], v[128:131], a[112:115]\n"
"s_sub_u32 s21, s21, 1\n"
"ds_read_b128 a[164:167], v241 offset:0\n"
"v_cvt_pk_bf16_f32 v157, v110, v111\n"
"v_smfmac_f32_16x16x64_bf16 a[128:131], a[144:147], v[128:135], v239\n"
"s_cbranch_scc0 L_body_%=\n"
"L_tail_%=:\n"
"v_smfmac_f32_16x16x64_bf16 a[132:135], a[144:147], v[136:143], v239\n"
"s_waitcnt lgkmcnt(1)\n"
"ds_read_b128 a[168:171], v240 offset:512\n"
"v_mfma_f32_16x16x32_bf16 v[64:67], a[160:163], v[0:3], v[160:163]\n"
"v_exp_f32 v112, v112\n"
"v_mfma_f32_16x16x32_bf16 v[68:71], a[160:163], v[4:7], v[164:167]\n"
"v_add_u32 v212, 65536, v249\n"
"v_xor_b32 v213, 32, v212\n"
"v_mfma_f32_16x16x32_bf16 v[72:75], a[160:163], v[8:11], v[168:171]\n"
"v_exp_f32 v113, v113\n"
"v_mfma_f32_16x16x32_bf16 v[76:79], a[160:163], v[12:15], v[172:175]\n"
"s_waitcnt lgkmcnt(1)\n"
"ds_read_b128 a[172:175], v241 offset:512\n"
"v_cvt_pk_bf16_f32 v146, v112, v113\n"
"v_mfma_f32_16x16x32_bf16 v[76:79], a[164:167], v[28:31], v[76:79]\n"
"v_exp_f32 v114, v114\n"
"v_mfma_f32_16x16x32_bf16 v[72:75], a[164:167], v[24:27], v[72:75]\n"
"v_xor_b32 v214, 64, v212\n"
"v_xor_b32 v215, 96, v212\n"
"v_mfma_f32_16x16x32_bf16 v[68:71], a[164:167], v[20:23], v[68:71]\n"
"v_exp_f32 v115, v115\n"
"v_mfma_f32_16x16x32_bf16 v[64:67], a[164:167], v[16:19], v[64:67]\n"
"s_waitcnt lgkmcnt(1)\n"
"ds_read_b128 a[160:163], v240 offset:4096\n"
"v_cvt_pk_bf16_f32 v147, v114, v115\n"
"v_mfma_f32_16x16x32_bf16 v[64:67], a[168:171], v[32:35], v[64:67]\n"
"v_exp_f32 v116, v116\n"
"v_mfma_f32_16x16x32_bf16 v[68:71], a[168:171], v[36:39], v[68:71]\n"
"buffer_store_dword a156, v253, s[60:63], s74 offen sc1\n"
"v_mfma_f32_16x16x32_bf16 v[72:75], a[168:171], v[40:43], v[72:75]\n"
"v_exp_f32 v117, v117\n"
"v_mfma_f32_16x16x32_bf16 v[76:79], a[168:171], v[44:47], v[76:79]\n"
"s_waitcnt lgkmcnt(1)\n"
"ds_read_b128 a[164:167], v241 offset:4096\n"
"v_cvt_pk_bf16_f32 v150, v116, v117\n"
"v_mfma_f32_16x16x32_bf16 v[76:79], a[172:175], v[60:63], v[76:79]\n"
"s_add_u32 m0, s23, 16384\n"
"v_exp_f32 v118, v118\n"
"v_mfma_f32_16x16x32_bf16 v[72:75], a[172:175], v[56:59], v[72:75]\n"
"buffer_load_dwordx4 v247, s[48:51], s26 offen nt lds\n"
"v_mfma_f32_16x16x32_bf16 v[68:71], a[172:175], v[52:55], v[68:71]\n"
"s_add_u32 s26, s26, 4096\n"
"v_exp_f32 v119, v119\n"
"v_mfma_f32_16x16x32_bf16 v[64:67], a[172:175], v[48:51], v[64:67]\n"
"s_waitcnt lgkmcnt(1)\n"
"ds_read_b128 a[168:171], v240 offset:4608\n"
"v_cvt_pk_bf16_f32 v151, v118, v119\n"
"v_mfma_f32_16x16x32_bf16 v[80:83], a[160:163], v[0:3], v[160:163]\n"
"s_add_u32 m0, s23, 20480\n"
"v_exp_f32 v120, v120\n"
"v_mfma_f32_16x16x32_bf16 v[84:87], a[160:163], v[4:7], v[164:167]\n"
"buffer_load_dwordx4 v247, s[48:51], s26 offen nt lds\n"
"v_mfma_f32_16x16x32_bf16 v[88:91], a[160:163], v[8:11], v[168:171]\n"
"s_add_u32 s26, s26, 4096\n"
"v_exp_f32 v121, v121\n"
"v_mfma_f32_16x16x32_bf16 v[92:95], a[160:163], v[12:15], v[172:175]\n"
"s_waitcnt lgkmcnt(1)\n"
"ds_read_b128 a[172:175], v241 offset:4608\n"
"v_cvt_pk_bf16_f32 v154, v120, v121\n"
"v_mfma_f32_16x16x32_bf16 v[92:95], a[164:167], v[28:31], v[92:95]\n"
"s_cmp_eq_u32 s24, 0\n"
"v_exp_f32 v122, v122\n"
"v_mfma_f32_16x16x32_bf16 v[88:91], a[164:167], v[24:27], v[88:91]\n"
"s_cselect_b32 s22, 2, -1\n"
"ds_read_b64_tr_b16 a[160:161], v242 offset:0\n"
"ds_read_b64_tr_b16 a[176:177], v212 offset:32768\n"
"v_mfma_f32_16x16x32_bf16 v[84:87], a[164:167], v[20:23], v[84:87]\n"
"s_lshl_b32 s22, s22, 15\n"
"v_exp_f32 v123, v123\n"
"v_mfma_f32_16x16x32_bf16 v[80:83], a[164:167], v[16:19], v[80:83]\n"
"s_waitcnt lgkmcnt(3)\n"
"ds_read_b64_tr_b16 a[162:163], v242 offset:4096\n"
"v_cvt_pk_bf16_f32 v155, v122, v123\n"
"v_mfma_f32_16x16x32_bf16 v[80:83], a[168:171], v[32:35], v[80:83]\n"
"v_exp_f32 v124, v124\n"
"v_mfma_f32_16x16x32_bf16 v[84:87], a[168:171], v[36:39], v[84:87]\n"
"ds_read_b64_tr_b16 a[164:165], v243 offset:0\n"
"ds_read_b64_tr_b16 a[178:179], v212 offset:34816\n"
"v_mfma_f32_16x16x32_bf16 v[88:91], a[168:171], v[40:43], v[88:91]\n"
"v_exp_f32 v125, v125\n"
"v_mfma_f32_16x16x32_bf16 v[92:95], a[168:171], v[44:47], v[92:95]\n"
"s_waitcnt lgkmcnt(5)\n"
"ds_read_b64_tr_b16 a[166:167], v243 offset:4096\n"
"v_cvt_pk_bf16_f32 v158, v124, v125\n"
"v_mfma_f32_16x16x32_bf16 v[92:95], a[172:175], v[60:63], v[92:95]\n"
"v_exp_f32 v126, v126\n"
"v_mfma_f32_16x16x32_bf16 v[88:91], a[172:175], v[56:59], v[88:91]\n"
"ds_read_b64_tr_b16 a[168:169], v244 offset:0\n"
"ds_read_b64_tr_b16 a[180:181], v213 offset:32768\n"
"v_mfma_f32_16x16x32_bf16 v[84:87], a[172:175], v[52:55], v[84:87]\n"
"v_exp_f32 v127, v127\n"
"v_mfma_f32_16x16x32_bf16 v[80:83], a[172:175], v[48:51], v[80:83]\n"
"s_waitcnt lgkmcnt(5)\n"
"ds_read_b64_tr_b16 a[170:171], v244 offset:4096\n"
"v_cvt_pk_bf16_f32 v159, v126, v127\n"
"v_mfma_f32_16x16x32_bf16 a[0:3], a[160:163], v[144:147], a[0:3]\n"
"v_exp_f32 v64, v64\n"
"v_mfma_f32_16x16x32_bf16 a[4:7], a[160:163], v[148:151], a[4:7]\n"
"ds_read_b64_tr_b16 a[172:173], v245 offset:0\n"
"ds_read_b64_tr_b16 a[182:183], v213 offset:34816\n"
"v_mfma_f32_16x16x32_bf16 a[8:11], a[160:163], v[152:155], a[8:11]\n"
"v_exp_f32 v65, v65\n"
"v_mfma_f32_16x16x32_bf16 a[12:15], a[160:163], v[156:159], a[12:15]\n"
"s_waitcnt lgkmcnt(5)\n"
"ds_read_b64_tr_b16 a[174:175], v245 offset:4096\n"
"v_cvt_pk_bf16_f32 v128, v64, v65\n"
"v_mfma_f32_16x16x32_bf16 a[28:31], a[164:167], v[156:159], a[28:31]\n"
"v_exp_f32 v66, v66\n"
"v_mfma_f32_16x16x32_bf16 a[24:27], a[164:167], v[152:155], a[24:27]\n"
"ds_read_b64_tr_b16 a[160:161], v242 offset:512\n"
"ds_read_b64_tr_b16 a[184:185], v214 offset:32768\n"
"v_mfma_f32_16x16x32_bf16 a[20:23], a[164:167], v[148:151], a[20:23]\n"
"v_exp_f32 v67, v67\n"
"v_mfma_f32_16x16x32_bf16 a[16:19], a[164:167], v[144:147], a[16:19]\n"
"s_waitcnt lgkmcnt(5)\n"
"ds_read_b64_tr_b16 a[162:163], v242 offset:4608\n"
"v_cvt_pk_bf16_f32 v129, v66, v67\n"
"v_mfma_f32_16x16x32_bf16 a[32:35], a[168:171], v[144:147], a[32:35]\n"
"v_exp_f32 v68, v68\n"
"v_mfma_f32_16x16x32_bf16 a[36:39], a[168:171], v[148:151], a[36:39]\n"
"ds_read_b64_tr_b16 a[164:165], v243 offset:512\n"
"ds_read_b64_tr_b16 a[186:187], v214 offset:34816\n"
"v_mfma_f32_16x16x32_bf16 a[40:43], a[168:171], v[152:155], a[40:43]\n"
"v_exp_f32 v69, v69\n"
"v_mfma_f32_16x16x32_bf16 a[44:47], a[168:171], v[156:159], a[44:47]\n"
"s_waitcnt lgkmcnt(5)\n"
"ds_read_b64_tr_b16 a[166:167], v243 offset:4608\n"
"v_cvt_pk_bf16_f32 v132, v68, v69\n"
"v_mfma_f32_16x16x32_bf16 a[60:63], a[172:175], v[156:159], a[60:63]\n"
"v_exp_f32 v70, v70\n"
"v_mfma_f32_16x16x32_bf16 a[56:59], a[172:175], v[152:155], a[56:59]\n"
"ds_read_b64_tr_b16 a[168:169], v244 offset:512\n"
"ds_read_b64_tr_b16 a[188:189], v215 offset:32768\n"
"v_mfma_f32_16x16x32_bf16 a[52:55], a[172:175], v[148:151], a[52:55]\n"
"v_exp_f32 v71, v71\n"
"v_mfma_f32_16x16x32_bf16 a[48:51], a[172:175], v[144:147], a[48:51]\n"
"s_waitcnt lgkmcnt(5)\n"
"ds_read_b64_tr_b16 a[170:171], v244 offset:4608\n"
"v_cvt_pk_bf16_f32 v133, v70, v71\n"
"v_mfma_f32_16x16x32_bf16 a[64:67], a[160:163], v[144:147], a[64:67]\n"
"v_exp_f32 v72, v72\n"
"v_mfma_f32_16x16x32_bf16 a[68:71], a[160:163], v[148:151], a[68:71]\n"
"ds_read_b64_tr_b16 a[172:173], v245 offset:512\n"
"ds_read_b64_tr_b16 a[190:191], v215 offset:34816\n"
"v_mfma_f32_16x16x32_bf16 a[72:75], a[160:163], v[152:155], a[72:75]\n"
"v_exp_f32 v73, v73\n"
"v_mfma_f32_16x16x32_bf16 a[76:79], a[160:163], v[156:159], a[76:79]\n"
"s_waitcnt lgkmcnt(5)\n"
"ds_read_b64_tr_b16 a[174:175], v245 offset:4608\n"
"v_cvt_pk_bf16_f32 v136, v72, v73\n"
"v_mfma_f32_16x16x32_bf16 a[92:95], a[164:167], v[156:159], a[92:95]\n"
"s_add_u32 m0, s33, 148480\n"
"v_exp_f32 v74, v74\n"
"v_mfma_f32_16x16x32_bf16 a[88:91], a[164:167], v[152:155], a[88:91]\n"
"s_add_u32 s72, s80, 106496\n"
"buffer_load_dwordx4 v248, s[56:59], s72 offen lds\n"
"v_mfma_f32_16x16x32_bf16 a[84:87], a[164:167], v[148:151], a[84:87]\n"
"v_exp_f32 v75, v75\n"
"v_mfma_f32_16x16x32_bf16 a[80:83], a[164:167], v[144:147], a[80:83]\n"
"s_waitcnt lgkmcnt(3)\n"
"v_cvt_pk_bf16_f32 v137, v74, v75\n"
"v_mfma_f32_16x16x32_bf16 a[96:99], a[168:171], v[144:147], a[96:99]\n"
"s_add_u32 m0, s33, 149504\n"
"v_exp_f32 v76, v76\n"
"v_mfma_f32_16x16x32_bf16 a[100:103], a[168:171], v[148:151], a[100:103]\n"
"s_add_u32 s72, s80, 114688\n"
"buffer_load_dwordx4 v248, s[56:59], s72 offen lds\n"
"v_mfma_f32_16x16x32_bf16 a[104:107], a[168:171], v[152:155], a[104:107]\n"
"v_exp_f32 v77, v77\n"
"v_mfma_f32_16x16x32_bf16 a[108:111], a[168:171], v[156:159], a[108:111]\n"
"s_waitcnt lgkmcnt(0)\n"
"ds_read_b128 a[160:163], v240 offset:8192\n"
"v_cvt_pk_bf16_f32 v140, v76, v77\n"
"v_mfma_f32_16x16x32_bf16 a[124:127], a[172:175], v[156:159], a[124:127]\n"
"s_add_u32 m0, s33, 150528\n"
"v_exp_f32 v78, v78\n"
"v_mfma_f32_16x16x32_bf16 a[120:123], a[172:175], v[152:155], a[120:123]\n"
"s_add_u32 s72, s80, 122880\n"
"buffer_load_dwordx4 v248, s[56:59], s72 offen lds\n"
"v_mfma_f32_16x16x32_bf16 a[116:119], a[172:175], v[148:151], a[116:119]\n"
"v_exp_f32 v79, v79\n"
"v_mfma_f32_16x16x32_bf16 a[112:115], a[172:175], v[144:147], a[112:115]\n"
"s_add_u32 m0, s33, 65536\n"
"ds_read_b128 a[164:167], v241 offset:8192\n"
"v_cvt_pk_bf16_f32 v141, v78, v79\n"
"v_smfmac_f32_16x16x64_bf16 a[128:131], a[144:147], v[144:151], v239\n"
"s_add_u32 s72, s80, 131072\n"
"buffer_load_dwordx4 v248, s[56:59], s72 offen lds\n"
"v_smfmac_f32_16x16x64_bf16 a[132:135], a[144:147], v[152:159], v239\n"
"s_waitcnt lgkmcnt(1)\n"
"ds_read_b128 a[168:171], v240 offset:8704\n"
"v_mfma_f32_16x16x32_bf16 v[96:99], a[160:163], v[0:3], v[160:163]\n"
"s_add_u32 m0, s33, 66560\n"
"v_exp_f32 v80, v80\n"
"v_mfma_f32_16x16x32_bf16 v[100:103], a[160:163], v[4:7], v[164:167]\n"
"s_add_u32 s72, s80, 139264\n"
"buffer_load_dwordx4 v248, s[56:59], s72 offen lds\n"
"v_mfma_f32_16x16x32_bf16 v[104:107], a[160:163], v[8:11], v[168:171]\n"
"v_exp_f32 v81, v81\n"
"v_mfma_f32_16x16x32_bf16 v[108:111], a[160:163], v[12:15], v[172:175]\n"
"s_waitcnt lgkmcnt(1)\n"
"ds_read_b128 a[172:175], v241 offset:8704\n"
"v_cvt_pk_bf16_f32 v130, v80, v81\n"
"v_mfma_f32_16x16x32_bf16 v[108:111], a[164:167], v[28:31], v[108:111]\n"
"s_add_u32 m0, s33, 67584\n"
"v_exp_f32 v82, v82\n"
"v_mfma_f32_16x16x32_bf16 v[104:107], a[164:167], v[24:27], v[104:107]\n"
"s_add_u32 s72, s80, 147456\n"
"buffer_load_dwordx4 v248, s[56:59], s72 offen lds\n"
"v_mfma_f32_16x16x32_bf16 v[100:103], a[164:167], v[20:23], v[100:103]\n"
"v_exp_f32 v83, v83\n"
"v_mfma_f32_16x16x32_bf16 v[96:99], a[164:167], v[16:19], v[96:99]\n"
"s_waitcnt lgkmcnt(1)\n"
"ds_read_b128 a[160:163], v240 offset:12288\n"
"v_cvt_pk_bf16_f32 v131, v82, v83\n"
"v_mfma_f32_16x16x32_bf16 v[96:99], a[168:171], v[32:35], v[96:99]\n"
"s_add_u32 m0, s33, 68608\n"
"v_exp_f32 v84, v84\n"
"v_mfma_f32_16x16x32_bf16 v[100:103], a[168:171], v[36:39], v[100:103]\n"
"s_add_u32 s72, s80, 155648\n"
"buffer_load_dwordx4 v248, s[56:59], s72 offen lds\n"
"v_mfma_f32_16x16x32_bf16 v[104:107], a[168:171], v[40:43], v[104:107]\n"
"v_exp_f32 v85, v85\n"
"v_mfma_f32_16x16x32_bf16 v[108:111], a[168:171], v[44:47], v[108:111]\n"
"s_waitcnt lgkmcnt(1)\n"
"ds_read_b128 a[164:167], v241 offset:12288\n"
"v_cvt_pk_bf16_f32 v134, v84, v85\n"
"v_mfma_f32_16x16x32_bf16 v[108:111], a[172:175], v[60:63], v[108:111]\n"
"s_add_u32 m0, s33, 98304\n"
"v_exp_f32 v86, v86\n"
"v_mfma_f32_16x16x32_bf16 v[104:107], a[172:175], v[56:59], v[104:107]\n"
"s_add_u32 s72, s80, 163840\n"
"buffer_load_dwordx4 v248, s[56:59], s72 offen lds\n"
"v_mfma_f32_16x16x32_bf16 v[100:103], a[172:175], v[52:55], v[100:103]\n"
"v_exp_f32 v87, v87\n"
"v_mfma_f32_16x16x32_bf16 v[96:99], a[172:175], v[48:51], v[96:99]\n"
"s_waitcnt lgkmcnt(1)\n"
"ds_read_b128 a[168:171], v240 offset:12800\n"
"v_cvt_pk_bf16_f32 v135, v86, v87\n"
"v_mfma_f32_16x16x32_bf16 v[112:115], a[160:163], v[0:3], v[160:163]\n"
"s_add_u32 m0, s33, 99328\n"
"v_exp_f32 v88, v88\n"
"v_mfma_f32_16x16x32_bf16 v[116:119], a[160:163], v[4:7], v[164:167]\n"
"s_add_u32 s72, s80, 172032\n"
"buffer_load_dwordx4 v248, s[56:59], s72 offen lds\n"
"v_mfma_f32_16x16x32_bf16 v[120:123], a[160:163], v[8:11], v[168:171]\n"
"v_exp_f32 v89, v89\n"
"v_mfma_f32_16x16x32_bf16 v[124:127], a[160:163], v[12:15], v[172:175]\n"
"s_waitcnt lgkmcnt(1)\n"
"ds_read_b128 a[172:175], v241 offset:12800\n"
"v_cvt_pk_bf16_f32 v138, v88, v89\n"
"v_mfma_f32_16x16x32_bf16 v[124:127], a[164:167], v[28:31], v[124:127]\n"
"s_add_u32 m0, s33, 100352\n"
"v_exp_f32 v90, v90\n"
"v_mfma_f32_16x16x32_bf16 v[120:123], a[164:167], v[24:27], v[120:123]\n"
"s_add_u32 s72, s80, 180224\n"
"ds_read_b64_tr_b16 a[160:161], v242 offset:8192\n"
"buffer_load_dwordx4 v248, s[56:59], s72 offen lds\n"
"v_mfma_f32_16x16x32_bf16 v[116:119], a[164:167], v[20:23], v[116:119]\n"
"v_exp_f32 v91, v91\n"
"v_mfma_f32_16x16x32_bf16 v[112:115], a[164:167], v[16:19], v[112:115]\n"
"s_waitcnt lgkmcnt(2)\n"
"ds_read_b64_tr_b16 a[162:163], v242 offset:12288\n"
"v_cvt_pk_bf16_f32 v139, v90, v91\n"
"v_mfma_f32_16x16x32_bf16 v[112:115], a[168:171], v[32:35], v[112:115]\n"
"v_exp_f32 v92, v92\n"
"v_mfma_f32_16x16x32_bf16 v[116:119], a[168:171], v[36:39], v[116:119]\n"
"ds_read_b64_tr_b16 a[164:165], v243 offset:8192\n"
"v_add_u32 v240, s22, v240\n"
"v_mfma_f32_16x16x32_bf16 v[120:123], a[168:171], v[40:43], v[120:123]\n"
"v_exp_f32 v93, v93\n"
"v_mfma_f32_16x16x32_bf16 v[124:127], a[168:171], v[44:47], v[124:127]\n"
"s_waitcnt lgkmcnt(3)\n"
"ds_read_b64_tr_b16 a[166:167], v243 offset:12288\n"
"v_cvt_pk_bf16_f32 v142, v92, v93\n"
"v_mfma_f32_16x16x32_bf16 v[124:127], a[172:175], v[60:63], v[124:127]\n"
"v_exp_f32 v94, v94\n"
"v_mfma_f32_16x16x32_bf16 v[120:123], a[172:175], v[56:59], v[120:123]\n"
"ds_read_b64_tr_b16 a[168:169], v244 offset:8192\n"
"v_add_u32 v241, s22, v241\n"
"v_mfma_f32_16x16x32_bf16 v[116:119], a[172:175], v[52:55], v[116:119]\n"
"v_exp_f32 v95, v95\n"
"v_mfma_f32_16x16x32_bf16 v[112:115], a[172:175], v[48:51], v[112:115]\n"
"s_waitcnt lgkmcnt(3)\n"
"ds_read_b64_tr_b16 a[170:171], v244 offset:12288\n"
"v_cvt_pk_bf16_f32 v143, v94, v95\n"
"v_mfma_f32_16x16x32_bf16 a[0:3], a[160:163], v[128:131], a[0:3]\n"
"s_add_u32 m0, s33, 101376\n"
"v_exp_f32 v96, v96\n"
"v_mfma_f32_16x16x32_bf16 a[4:7], a[160:163], v[132:135], a[4:7]\n"
"s_add_u32 s72, s80, 188416\n"
"ds_read_b64_tr_b16 a[172:173], v245 offset:8192\n"
"buffer_load_dwordx4 v248, s[56:59], s72 offen lds\n"
"v_mfma_f32_16x16x32_bf16 a[8:11], a[160:163], v[136:139], a[8:11]\n"
"v_exp_f32 v97, v97\n"
"v_mfma_f32_16x16x32_bf16 a[12:15], a[160:163], v[140:143], a[12:15]\n"
"s_waitcnt lgkmcnt(3)\n"
"ds_read_b64_tr_b16 a[174:175], v245 offset:12288\n"
"v_cvt_pk_bf16_f32 v144, v96, v97\n"
"v_mfma_f32_16x16x32_bf16 a[28:31], a[164:167], v[140:143], a[28:31]\n"
"v_exp_f32 v98, v98\n"
"v_mfma_f32_16x16x32_bf16 a[24:27], a[164:167], v[136:139], a[24:27]\n"
"ds_read_b64_tr_b16 a[160:161], v242 offset:8704\n"
"v_mfma_f32_16x16x32_bf16 a[20:23], a[164:167], v[132:135], a[20:23]\n"
"v_exp_f32 v99, v99\n"
"v_mfma_f32_16x16x32_bf16 a[16:19], a[164:167], v[128:131], a[16:19]\n"
"s_waitcnt lgkmcnt(3)\n"
"ds_read_b64_tr_b16 a[162:163], v242 offset:12800\n"
"v_cvt_pk_bf16_f32 v145, v98, v99\n"
"v_mfma_f32_16x16x32_bf16 a[32:35], a[168:171], v[128:131], a[32:35]\n"
"v_exp_f32 v100, v100\n"
"v_mfma_f32_16x16x32_bf16 a[36:39], a[168:171], v[132:135], a[36:39]\n"
"ds_read_b64_tr_b16 a[164:165], v243 offset:8704\n"
"v_add_u32 v242, s22, v242\n"
"v_mfma_f32_16x16x32_bf16 a[40:43], a[168:171], v[136:139], a[40:43]\n"
"v_exp_f32 v101, v101\n"
"v_mfma_f32_16x16x32_bf16 a[44:47], a[168:171], v[140:143], a[44:47]\n"
"s_waitcnt lgkmcnt(3)\n"
"ds_read_b64_tr_b16 a[166:167], v243 offset:12800\n"
"v_cvt_pk_bf16_f32 v148, v100, v101\n"
"v_mfma_f32_16x16x32_bf16 a[60:63], a[172:175], v[140:143], a[60:63]\n"
"v_exp_f32 v102, v102\n"
"v_mfma_f32_16x16x32_bf16 a[56:59], a[172:175], v[136:139], a[56:59]\n"
"ds_read_b64_tr_b16 a[168:169], v244 offset:8704\n"
"v_add_u32 v243, s22, v243\n"
"v_mfma_f32_16x16x32_bf16 a[52:55], a[172:175], v[132:135], a[52:55]\n"
"v_exp_f32 v103, v103\n"
"v_mfma_f32_16x16x32_bf16 a[48:51], a[172:175], v[128:131], a[48:51]\n"
"s_waitcnt lgkmcnt(3)\n"
"ds_read_b64_tr_b16 a[170:171], v244 offset:12800\n"
"v_cvt_pk_bf16_f32 v149, v102, v103\n"
"v_mfma_f32_16x16x32_bf16 a[64:67], a[160:163], v[128:131], a[64:67]\n"
"v_exp_f32 v104, v104\n"
"v_mfma_f32_16x16x32_bf16 a[68:71], a[160:163], v[132:135], a[68:71]\n"
"ds_read_b64_tr_b16 a[172:173], v245 offset:8704\n"
"v_add_u32 v244, s22, v244\n"
"v_mfma_f32_16x16x32_bf16 a[72:75], a[160:163], v[136:139], a[72:75]\n"
"v_exp_f32 v105, v105\n"
"v_mfma_f32_16x16x32_bf16 a[76:79], a[160:163], v[140:143], a[76:79]\n"
"s_waitcnt lgkmcnt(3)\n"
"ds_read_b64_tr_b16 a[174:175], v245 offset:12800\n"
"v_cvt_pk_bf16_f32 v152, v104, v105\n"
"v_mfma_f32_16x16x32_bf16 a[92:95], a[164:167], v[140:143], a[92:95]\n"
"v_exp_f32 v106, v106\n"
"v_mfma_f32_16x16x32_bf16 a[88:91], a[164:167], v[136:139], a[88:91]\n"
"v_add_u32 v245, s22, v245\n"
"v_mfma_f32_16x16x32_bf16 a[84:87], a[164:167], v[132:135], a[84:87]\n"
"v_exp_f32 v107, v107\n"
"v_mfma_f32_16x16x32_bf16 a[80:83], a[164:167], v[128:131], a[80:83]\n"
"s_waitcnt lgkmcnt(2)\n"
"v_cvt_pk_bf16_f32 v153, v106, v107\n"
"v_mfma_f32_16x16x32_bf16 a[96:99], a[168:171], v[128:131], a[96:99]\n"
"v_exp_f32 v108, v108\n"
"v_mfma_f32_16x16x32_bf16 a[100:103], a[168:171], v[132:135], a[100:103]\n"
"s_waitcnt vmcnt(11) lgkmcnt(0)\n"
"s_barrier\n"
"v_mfma_f32_16x16x32_bf16 a[104:107], a[168:171], v[136:139], a[104:107]\n"
"v_exp_f32 v109, v109\n"
"v_mfma_f32_16x16x32_bf16 a[108:111], a[168:171], v[140:143], a[108:111]\n"
"ds_read_b128 a[160:163], v240 offset:0\n"
"v_cvt_pk_bf16_f32 v156, v108, v109\n"
"v_mfma_f32_16x16x32_bf16 a[124:127], a[172:175], v[140:143], a[124:127]\n"
"v_exp_f32 v110, v110\n"
"v_mfma_f32_16x16x32_bf16 a[120:123], a[172:175], v[136:139], a[120:123]\n"
"v_mfma_f32_16x16x32_bf16 a[116:119], a[172:175], v[132:135], a[116:119]\n"
"v_exp_f32 v111, v111\n"
"v_mfma_f32_16x16x32_bf16 a[112:115], a[172:175], v[128:131], a[112:115]\n"
"s_add_u32 m0, s33, 32768\n"
"ds_read_b128 a[164:167], v241 offset:0\n"
"v_cvt_pk_bf16_f32 v157, v110, v111\n"
"v_smfmac_f32_16x16x64_bf16 a[128:131], a[144:147], v[128:135], v239\n"
"s_add_u32 s72, s80, 196608\n"
"buffer_load_dwordx4 v248, s[56:59], s72 offen lds\n"
"v_smfmac_f32_16x16x64_bf16 a[132:135], a[144:147], v[136:143], v239\n"
"s_waitcnt lgkmcnt(1)\n"
"ds_read_b128 a[168:171], v240 offset:512\n"
"ds_read_b128 v[220:223], v240 offset:8192\n"
"v_mfma_f32_16x16x32_bf16 v[64:67], a[160:163], v[0:3], v[160:163]\n"
"s_add_u32 m0, s33, 33792\n"
"v_exp_f32 v112, v112\n"
"v_mfma_f32_16x16x32_bf16 v[68:71], a[160:163], v[4:7], v[164:167]\n"
"s_add_u32 s72, s80, 204800\n"
"buffer_load_dwordx4 v248, s[56:59], s72 offen lds\n"
"v_mfma_f32_16x16x32_bf16 v[72:75], a[160:163], v[8:11], v[168:171]\n"
"v_exp_f32 v113, v113\n"
"v_mfma_f32_16x16x32_bf16 v[76:79], a[160:163], v[12:15], v[172:175]\n"
"s_waitcnt lgkmcnt(2)\n"
"ds_read_b128 a[172:175], v241 offset:512\n"
"v_cvt_pk_bf16_f32 v146, v112, v113\n"
"v_mfma_f32_16x16x32_bf16 v[76:79], a[164:167], v[28:31], v[76:79]\n"
"s_add_u32 m0, s33, 34816\n"
"v_exp_f32 v114, v114\n"
"v_mfma_f32_16x16x32_bf16 v[72:75], a[164:167], v[24:27], v[72:75]\n"
"s_add_u32 s72, s80, 212992\n"
"buffer_load_dwordx4 v248, s[56:59], s72 offen lds\n"
"v_mfma_f32_16x16x32_bf16 v[68:71], a[164:167], v[20:23], v[68:71]\n"
"v_exp_f32 v115, v115\n"
"v_mfma_f32_16x16x32_bf16 v[64:67], a[164:167], v[16:19], v[64:67]\n"
"s_waitcnt lgkmcnt(2)\n"
"ds_read_b128 a[160:163], v240 offset:4096\n"
"v_cvt_pk_bf16_f32 v147, v114, v115\n"
"v_mfma_f32_16x16x32_bf16 v[64:67], a[168:171], v[32:35], v[64:67]\n"
"s_add_u32 m0, s33, 35840\n"
"v_exp_f32 v116, v116\n"
"v_mfma_f32_16x16x32_bf16 v[68:71], a[168:171], v[36:39], v[68:71]\n"
"s_add_u32 s72, s80, 221184\n"
"buffer_load_dwordx4 v248, s[56:59], s72 offen lds\n"
"v_mfma_f32_16x16x32_bf16 v[72:75], a[168:171], v[40:43], v[72:75]\n"
"v_exp_f32 v117, v117\n"
"v_mfma_f32_16x16x32_bf16 v[76:79], a[168:171], v[44:47], v[76:79]\n"
"s_waitcnt lgkmcnt(1)\n"
"ds_read_b128 a[164:167], v241 offset:4096\n"
"v_cvt_pk_bf16_f32 v150, v116, v117\n"
"v_mfma_f32_16x16x32_bf16 v[76:79], a[172:175], v[60:63], v[76:79]\n"
"s_add_u32 m0, s33, 49152\n"
"v_exp_f32 v118, v118\n"
"v_mfma_f32_16x16x32_bf16 v[72:75], a[172:175], v[56:59], v[72:75]\n"
"s_add_u32 s72, s80, 229376\n"
"buffer_load_dwordx4 v248, s[56:59], s72 offen lds\n"
"v_mfma_f32_16x16x32_bf16 v[68:71], a[172:175], v[52:55], v[68:71]\n"
"v_exp_f32 v119, v119\n"
"v_mfma_f32_16x16x32_bf16 v[64:67], a[172:175], v[48:51], v[64:67]\n"
"s_waitcnt lgkmcnt(1)\n"
"ds_read_b128 a[168:171], v240 offset:4608\n"
"v_cvt_pk_bf16_f32 v151, v118, v119\n"
"v_mfma_f32_16x16x32_bf16 v[80:83], a[160:163], v[0:3], v[160:163]\n"
"s_add_u32 m0, s33, 50176\n"
"v_exp_f32 v120, v120\n"
"v_mfma_f32_16x16x32_bf16 v[84:87], a[160:163], v[4:7], v[164:167]\n"
"s_add_u32 s72, s80, 237568\n"
"buffer_load_dwordx4 v248, s[56:59], s72 offen lds\n"
"v_mfma_f32_16x16x32_bf16 v[88:91], a[160:163], v[8:11], v[168:171]\n"
"v_exp_f32 v121, v121\n"
"v_mfma_f32_16x16x32_bf16 v[92:95], a[160:163], v[12:15], v[172:175]\n"
"s_waitcnt lgkmcnt(1)\n"
"ds_read_b128 a[172:175], v241 offset:4608\n"
"ds_read_b128 v[224:227], v241 offset:8192\n"
"v_cvt_pk_bf16_f32 v154, v120, v121\n"
"v_mfma_f32_16x16x32_bf16 v[92:95], a[164:167], v[28:31], v[92:95]\n"
"v_exp_f32 v122, v122\n"
"v_mfma_f32_16x16x32_bf16 v[88:91], a[164:167], v[24:27], v[88:91]\n"
"ds_read_b64_tr_b16 a[160:161], v242 offset:0\n"
"ds_read_b128 v[228:231], v240 offset:8704\n"
"v_add_u32 v208, 65536, v242\n"
"v_mfma_f32_16x16x32_bf16 v[84:87], a[164:167], v[20:23], v[84:87]\n"
"v_exp_f32 v123, v123\n"
"v_mfma_f32_16x16x32_bf16 v[80:83], a[164:167], v[16:19], v[80:83]\n"
"s_waitcnt lgkmcnt(4)\n"
"ds_read_b64_tr_b16 a[162:163], v242 offset:4096\n"
"ds_read_b128 v[232:235], v241 offset:8704\n"
"v_cvt_pk_bf16_f32 v155, v122, v123\n"
"v_mfma_f32_16x16x32_bf16 v[80:83], a[168:171], v[32:35], v[80:83]\n"
"v_exp_f32 v124, v124\n"
"v_mfma_f32_16x16x32_bf16 v[84:87], a[168:171], v[36:39], v[84:87]\n"
"ds_read_b64_tr_b16 a[164:165], v243 offset:0\n"
"ds_read_b64_tr_b16 a[192:193], v242 offset:8192\n"
"v_add_u32 v209, 65536, v243\n"
"v_mfma_f32_16x16x32_bf16 v[88:91], a[168:171], v[40:43], v[88:91]\n"
"v_exp_f32 v125, v125\n"
"v_mfma_f32_16x16x32_bf16 v[92:95], a[168:171], v[44:47], v[92:95]\n"
"s_waitcnt lgkmcnt(7)\n"
"ds_read_b64_tr_b16 a[166:167], v243 offset:4096\n"
"ds_read_b64_tr_b16 a[194:195], v242 offset:12288\n"
"v_cvt_pk_bf16_f32 v158, v124, v125\n"
"v_mfma_f32_16x16x32_bf16 v[92:95], a[172:175], v[60:63], v[92:95]\n"
"v_exp_f32 v126, v126\n"
"v_mfma_f32_16x16x32_bf16 v[88:91], a[172:175], v[56:59], v[88:91]\n"
"ds_read_b64_tr_b16 a[168:169], v244 offset:0\n"
"ds_read_b64_tr_b16 a[196:197], v243 offset:8192\n"
"v_add_u32 v210, 65536, v244\n"
"v_mfma_f32_16x16x32_bf16 v[84:87], a[172:175], v[52:55], v[84:87]\n"
"v_exp_f32 v127, v127\n"
"v_mfma_f32_16x16x32_bf16 v[80:83], a[172:175], v[48:51], v[80:83]\n"
"s_waitcnt lgkmcnt(7)\n"
"ds_read_b64_tr_b16 a[170:171], v244 offset:4096\n"
"ds_read_b64_tr_b16 a[198:199], v243 offset:12288\n"
"v_cvt_pk_bf16_f32 v159, v126, v127\n"
"v_mfma_f32_16x16x32_bf16 a[0:3], a[160:163], v[144:147], a[0:3]\n"
"v_exp_f32 v64, v64\n"
"v_mfma_f32_16x16x32_bf16 a[4:7], a[160:163], v[148:151], a[4:7]\n"
"ds_read_b64_tr_b16 a[172:173], v245 offset:0\n"
"ds_read_b64_tr_b16 a[200:201], v244 offset:8192\n"
"v_add_u32 v211, 65536, v245\n"
"v_mfma_f32_16x16x32_bf16 a[8:11], a[160:163], v[152:155], a[8:11]\n"
"v_exp_f32 v65, v65\n"
"v_mfma_f32_16x16x32_bf16 a[12:15], a[160:163], v[156:159], a[12:15]\n"
"s_waitcnt lgkmcnt(7)\n"
"ds_read_b64_tr_b16 a[174:175], v245 offset:4096\n"
"ds_read_b64_tr_b16 a[202:203], v244 offset:12288\n"
"v_cvt_pk_bf16_f32 v176, v64, v65\n"
"v_mfma_f32_16x16x32_bf16 a[28:31], a[164:167], v[156:159], a[28:31]\n"
"v_exp_f32 v66, v66\n"
"v_mfma_f32_16x16x32_bf16 a[24:27], a[164:167], v[152:155], a[24:27]\n"
"ds_read_b64_tr_b16 a[160:161], v242 offset:512\n"
"ds_read_b64_tr_b16 a[204:205], v245 offset:8192\n"
"v_mfma_f32_16x16x32_bf16 a[20:23], a[164:167], v[148:151], a[20:23]\n"
"v_exp_f32 v67, v67\n"
"v_mfma_f32_16x16x32_bf16 a[16:19], a[164:167], v[144:147], a[16:19]\n"
"s_waitcnt lgkmcnt(7)\n"
"ds_read_b64_tr_b16 a[162:163], v242 offset:4608\n"
"ds_read_b64_tr_b16 a[206:207], v245 offset:12288\n"
"v_cvt_pk_bf16_f32 v177, v66, v67\n"
"v_mfma_f32_16x16x32_bf16 a[32:35], a[168:171], v[144:147], a[32:35]\n"
"v_exp_f32 v68, v68\n"
"v_mfma_f32_16x16x32_bf16 a[36:39], a[168:171], v[148:151], a[36:39]\n"
"ds_read_b64_tr_b16 a[164:165], v243 offset:512\n"
"ds_read_b64_tr_b16 a[208:209], v242 offset:8704\n"
"v_mfma_f32_16x16x32_bf16 a[40:43], a[168:171], v[152:155], a[40:43]\n"
"v_exp_f32 v69, v69\n"
"v_mfma_f32_16x16x32_bf16 a[44:47], a[168:171], v[156:159], a[44:47]\n"
"s_waitcnt lgkmcnt(7)\n"
"ds_read_b64_tr_b16 a[166:167], v243 offset:4608\n"
"ds_read_b64_tr_b16 a[210:211], v242 offset:12800\n"
"v_cvt_pk_bf16_f32 v184, v68, v69\n"
"v_mfma_f32_16x16x32_bf16 a[60:63], a[172:175], v[156:159], a[60:63]\n"
"v_exp_f32 v70, v70\n"
"v_mfma_f32_16x16x32_bf16 a[56:59], a[172:175], v[152:155], a[56:59]\n"
"ds_read_b64_tr_b16 a[168:169], v244 offset:512\n"
"ds_read_b64_tr_b16 a[212:213], v243 offset:8704\n"
"v_mfma_f32_16x16x32_bf16 a[52:55], a[172:175], v[148:151], a[52:55]\n"
"v_exp_f32 v71, v71\n"
"v_mfma_f32_16x16x32_bf16 a[48:51], a[172:175], v[144:147], a[48:51]\n"
"s_waitcnt lgkmcnt(7)\n"
"ds_read_b64_tr_b16 a[170:171], v244 offset:4608\n"
"ds_read_b64_tr_b16 a[214:215], v243 offset:12800\n"
"v_cvt_pk_bf16_f32 v185, v70, v71\n"
"v_mfma_f32_16x16x32_bf16 a[64:67], a[160:163], v[144:147], a[64:67]\n"
"v_exp_f32 v72, v72\n"
"v_mfma_f32_16x16x32_bf16 a[68:71], a[160:163], v[148:151], a[68:71]\n"
"ds_read_b64_tr_b16 a[172:173], v245 offset:512\n"
"ds_read_b64_tr_b16 a[216:217], v244 offset:8704\n"
"v_mfma_f32_16x16x32_bf16 a[72:75], a[160:163], v[152:155], a[72:75]\n"
"v_exp_f32 v73, v73\n"
"v_mfma_f32_16x16x32_bf16 a[76:79], a[160:163], v[156:159], a[76:79]\n"
"s_waitcnt lgkmcnt(7)\n"
"ds_read_b64_tr_b16 a[174:175], v245 offset:4608\n"
"ds_read_b64_tr_b16 a[218:219], v244 offset:12800\n"
"v_cvt_pk_bf16_f32 v192, v72, v73\n"
"v_mfma_f32_16x16x32_bf16 a[92:95], a[164:167], v[156:159], a[92:95]\n"
"s_add_u32 m0, s33, 51200\n"
"v_exp_f32 v74, v74\n"
"v_mfma_f32_16x16x32_bf16 a[88:91], a[164:167], v[152:155], a[88:91]\n"
"s_add_u32 s72, s80, 245760\n"
"buffer_load_dwordx4 v248, s[56:59], s72 offen lds\n"
"v_mfma_f32_16x16x32_bf16 a[84:87], a[164:167], v[148:151], a[84:87]\n"
"v_exp_f32 v75, v75\n"
"v_mfma_f32_16x16x32_bf16 a[80:83], a[164:167], v[144:147], a[80:83]\n"
"s_waitcnt lgkmcnt(5)\n"
"ds_read_b128 a[160:163], v240 offset:12288\n"
"ds_read_b64_tr_b16 a[220:221], v245 offset:8704\n"
"v_cvt_pk_bf16_f32 v193, v74, v75\n"
"v_mfma_f32_16x16x32_bf16 a[96:99], a[168:171], v[144:147], a[96:99]\n"
"s_add_u32 m0, s33, 52224\n"
"v_exp_f32 v76, v76\n"
"v_mfma_f32_16x16x32_bf16 a[100:103], a[168:171], v[148:151], a[100:103]\n"
"s_add_u32 s72, s80, 253952\n"
"buffer_load_dwordx4 v248, s[56:59], s72 offen lds\n"
"v_mfma_f32_16x16x32_bf16 a[104:107], a[168:171], v[152:155], a[104:107]\n"
"v_exp_f32 v77, v77\n"
"v_mfma_f32_16x16x32_bf16 a[108:111], a[168:171], v[156:159], a[108:111]\n"
"s_waitcnt lgkmcnt(3)\n"
"ds_read_b128 a[164:167], v241 offset:12288\n"
"ds_read_b64_tr_b16 a[222:223], v245 offset:12800\n"
"v_cvt_pk_bf16_f32 v200, v76, v77\n"
"v_mfma_f32_16x16x32_bf16 a[124:127], a[172:175], v[156:159], a[124:127]\n"
"v_exp_f32 v78, v78\n"
"v_mfma_f32_16x16x32_bf16 a[120:123], a[172:175], v[152:155], a[120:123]\n"
"buffer_load_dword v255, v254, s[60:63], s75 offen sc1\n"
"v_mfma_f32_16x16x32_bf16 a[116:119], a[172:175], v[148:151], a[116:119]\n"
"v_exp_f32 v79, v79\n"
"v_mfma_f32_16x16x32_bf16 a[112:115], a[172:175], v[144:147], a[112:115]\n"
"ds_read_b128 a[168:171], v240 offset:12800\n"
"ds_read_b64_tr_b16 a[224:225], v208 offset:0\n"
"v_cvt_pk_bf16_f32 v201, v78, v79\n"
"v_smfmac_f32_16x16x64_bf16 a[128:131], a[144:147], v[144:151], v239\n"
"ds_read_b128 a[172:175], v241 offset:12800\n"
"ds_read_b64_tr_b16 a[226:227], v208 offset:4096\n"
"v_smfmac_f32_16x16x64_bf16 a[132:135], a[144:147], v[152:159], v239\n"
"ds_read_b64_tr_b16 a[228:229], v209 offset:0\n"
"ds_read_b64_tr_b16 a[230:231], v209 offset:4096\n"
"v_mfma_f32_16x16x32_bf16 v[96:99], v[220:223], v[0:3], v[160:163]\n"
"s_lshl_b32 s68, s28, 2\n"
"v_exp_f32 v80, v80\n"
"v_mfma_f32_16x16x32_bf16 v[96:99], v[224:227], v[16:19], v[96:99]\n"
"s_add_u32 s68, s68, s29\n"
"v_exp_f32 v81, v81\n"
"v_mfma_f32_16x16x32_bf16 v[96:99], v[228:231], v[32:35], v[96:99]\n"
"s_lshl1_add_u32 s68, s68, s35\n"
"v_exp_f32 v82, v82\n"
"v_mfma_f32_16x16x32_bf16 v[96:99], v[232:235], v[48:51], v[96:99]\n"
"s_waitcnt lgkmcnt(9)\n"
"s_xor_b32 s68, s68, 1\n"
"v_exp_f32 v83, v83\n"
"v_mfma_f32_16x16x32_bf16 v[112:115], a[160:163], v[0:3], v[160:163]\n"
"s_waitcnt lgkmcnt(7)\n"
"ds_read_b64_tr_b16 a[232:233], v210 offset:0\n"
"v_cvt_pk_bf16_f32 v178, v80, v81\n"
"v_mfma_f32_16x16x32_bf16 v[112:115], a[164:167], v[16:19], v[112:115]\n"
"s_waitcnt lgkmcnt(6)\n"
"ds_read_b64_tr_b16 a[234:235], v210 offset:4096\n"
"v_cvt_pk_bf16_f32 v179, v82, v83\n"
"v_mfma_f32_16x16x32_bf16 v[112:115], a[168:171], v[32:35], v[112:115]\n"
"s_waitcnt lgkmcnt(5)\n"
"ds_read_b64_tr_b16 a[236:237], v211 offset:0\n"
"ds_read_b64_tr_b16 a[238:239], v211 offset:4096\n"
"v_mfma_f32_16x16x32_bf16 v[112:115], a[172:175], v[48:51], v[112:115]\n"
"ds_read_b64_tr_b16 a[240:241], v208 offset:512\n"
"ds_read_b64_tr_b16 a[242:243], v208 offset:4608\n"
"v_mfma_f32_16x16x32_bf16 a[0:3], a[192:195], v[176:179], a[0:3]\n"
"s_lshl_b32 s25, s68, 13\n"
"ds_read_b64_tr_b16 a[244:245], v209 offset:512\n"
"v_exp_f32 v96, v96\n"
"v_mfma_f32_16x16x32_bf16 a[16:19], a[196:199], v[176:179], a[16:19]\n"
"s_xor_b32 s26, s25, 4096\n"
"ds_read_b64_tr_b16 a[246:247], v209 offset:4608\n"
"v_exp_f32 v97, v97\n"
"v_mfma_f32_16x16x32_bf16 a[32:35], a[200:203], v[176:179], a[32:35]\n"
"s_xor_b32 s22, s25, 8192\n"
"ds_read_b64_tr_b16 a[248:249], v210 offset:512\n"
"v_exp_f32 v98, v98\n"
"v_mfma_f32_16x16x32_bf16 a[48:51], a[204:207], v[176:179], a[48:51]\n"
"s_xor_b32 s23, s25, 12288\n"
"ds_read_b64_tr_b16 a[250:251], v210 offset:4608\n"
"v_exp_f32 v99, v99\n"
"v_mfma_f32_16x16x32_bf16 a[64:67], a[208:211], v[176:179], a[64:67]\n"
"ds_read_b64_tr_b16 a[252:253], v211 offset:512\n"
"v_exp_f32 v112, v112\n"
"v_mfma_f32_16x16x32_bf16 a[80:83], a[212:215], v[176:179], a[80:83]\n"
"ds_read_b64_tr_b16 a[254:255], v211 offset:4608\n"
"v_exp_f32 v113, v113\n"
"v_mfma_f32_16x16x32_bf16 a[96:99], a[216:219], v[176:179], a[96:99]\n"
"v_exp_f32 v114, v114\n"
"v_mfma_f32_16x16x32_bf16 a[112:115], a[220:223], v[176:179], a[112:115]\n"
"v_exp_f32 v115, v115\n"
"v_mov_b32 v212, 0\n"
"v_mov_b32 v213, 0\n"
"v_mov_b32 v214, 0\n"
"v_mov_b32 v215, 0\n"
"v_cvt_pk_bf16_f32 v180, v96, v97\n"
"v_cvt_pk_bf16_f32 v181, v98, v99\n"
"v_cvt_pk_bf16_f32 v182, v112, v113\n"
"v_cvt_pk_bf16_f32 v183, v114, v115\n"
"s_nop 1\n"
"v_smfmac_f32_16x16x64_bf16 v[212:215], a[144:147], v[176:183], v239\n"
"s_waitcnt lgkmcnt(14)\n"
"v_accvgpr_read_b32 v64, a128\n"
"v_accvgpr_read_b32 v65, a130\n"
"v_mfma_f32_16x16x32_bf16 a[0:3], a[224:227], v[180:183], a[0:3]\n"
"s_waitcnt lgkmcnt(12)\n"
"v_accvgpr_read_b32 v66, a132\n"
"v_accvgpr_read_b32 v67, a134\n"
"v_mfma_f32_16x16x32_bf16 a[16:19], a[228:231], v[180:183], a[16:19]\n"
"s_waitcnt lgkmcnt(10)\n"
"v_accvgpr_read_b32 v70, a129\n"
"v_accvgpr_read_b32 v71, a131\n"
"v_mfma_f32_16x16x32_bf16 a[32:35], a[232:235], v[180:183], a[32:35]\n"
"s_waitcnt lgkmcnt(8)\n"
"v_accvgpr_read_b32 v72, a133\n"
"v_accvgpr_read_b32 v73, a135\n"
"v_mfma_f32_16x16x32_bf16 a[48:51], a[236:239], v[180:183], a[48:51]\n"
"s_waitcnt lgkmcnt(6)\n"
"v_add_f32 v64, v64, v70\n"
"v_add_f32 v65, v65, v71\n"
"v_mfma_f32_16x16x32_bf16 a[64:67], a[240:243], v[180:183], a[64:67]\n"
"s_waitcnt lgkmcnt(4)\n"
"v_add_f32 v66, v66, v72\n"
"v_add_f32 v67, v67, v73\n"
"v_mfma_f32_16x16x32_bf16 a[80:83], a[244:247], v[180:183], a[80:83]\n"
"s_waitcnt lgkmcnt(2)\n"
"v_add_f32 v212, v212, v213\n"
"v_add_f32 v214, v214, v215\n"
"v_mfma_f32_16x16x32_bf16 a[96:99], a[248:251], v[180:183], a[96:99]\n"
"s_waitcnt lgkmcnt(0)\n"
"v_add_f32 v64, v64, v212\n"
"v_add_f32 v64, v64, v214\n"
"v_mfma_f32_16x16x32_bf16 a[112:115], a[252:255], v[180:183], a[112:115]\n"
"v_mov_b32 v69, v64\n"
"v_rcp_f32 v64, v64\n"
"v_accvgpr_read_b32 v128, a0\n"
"v_accvgpr_read_b32 v129, a1\n"
"v_accvgpr_read_b32 v130, a2\n"
"v_accvgpr_read_b32 v131, a3\n"
"v_accvgpr_read_b32 v132, a16\n"
"v_accvgpr_read_b32 v133, a17\n"
"v_accvgpr_read_b32 v134, a18\n"
"v_accvgpr_read_b32 v135, a19\n"
"v_mul_f32 v128, v64, v128\n"
"v_mul_f32 v129, v64, v129\n"
"v_mul_f32 v130, v64, v130\n"
"v_mul_f32 v131, v64, v131\n"
"v_mul_f32 v132, v64, v132\n"
"v_mul_f32 v133, v64, v133\n"
"v_mul_f32 v134, v64, v134\n"
"v_mul_f32 v135, v64, v135\n"
"v_cvt_pk_bf16_f32 v144, v128, v129\n"
"v_cvt_pk_bf16_f32 v145, v130, v131\n"
"v_cvt_pk_bf16_f32 v146, v132, v133\n"
"v_cvt_pk_bf16_f32 v147, v134, v135\n"
"buffer_store_dwordx4 v[144:147], v251, s[52:55], s25 offen sc1\n"
"v_accvgpr_read_b32 v136, a32\n"
"v_accvgpr_read_b32 v137, a33\n"
"v_accvgpr_read_b32 v138, a34\n"
"v_accvgpr_read_b32 v139, a35\n"
"v_accvgpr_read_b32 v140, a48\n"
"v_accvgpr_read_b32 v141, a49\n"
"v_accvgpr_read_b32 v142, a50\n"
"v_accvgpr_read_b32 v143, a51\n"
"v_mul_f32 v136, v64, v136\n"
"v_mul_f32 v137, v64, v137\n"
"v_mul_f32 v138, v64, v138\n"
"v_mul_f32 v139, v64, v139\n"
"v_mul_f32 v140, v64, v140\n"
"v_mul_f32 v141, v64, v141\n"
"v_mul_f32 v142, v64, v142\n"
"v_mul_f32 v143, v64, v143\n"
"v_cvt_pk_bf16_f32 v148, v136, v137\n"
"v_cvt_pk_bf16_f32 v149, v138, v139\n"
"v_cvt_pk_bf16_f32 v150, v140, v141\n"
"v_cvt_pk_bf16_f32 v151, v142, v143\n"
"buffer_store_dwordx4 v[148:151], v251, s[52:55], s25 offen offset:1024 sc1\n"
"v_accvgpr_read_b32 v128, a64\n"
"v_accvgpr_read_b32 v129, a65\n"
"v_accvgpr_read_b32 v130, a66\n"
"v_accvgpr_read_b32 v131, a67\n"
"v_accvgpr_read_b32 v132, a80\n"
"v_accvgpr_read_b32 v133, a81\n"
"v_accvgpr_read_b32 v134, a82\n"
"v_accvgpr_read_b32 v135, a83\n"
"v_mul_f32 v128, v64, v128\n"
"v_mul_f32 v129, v64, v129\n"
"v_mul_f32 v130, v64, v130\n"
"v_mul_f32 v131, v64, v131\n"
"v_mul_f32 v132, v64, v132\n"
"v_mul_f32 v133, v64, v133\n"
"v_mul_f32 v134, v64, v134\n"
"v_mul_f32 v135, v64, v135\n"
"v_cvt_pk_bf16_f32 v152, v128, v129\n"
"v_cvt_pk_bf16_f32 v153, v130, v131\n"
"v_cvt_pk_bf16_f32 v154, v132, v133\n"
"v_cvt_pk_bf16_f32 v155, v134, v135\n"
"buffer_store_dwordx4 v[152:155], v251, s[52:55], s25 offen offset:2048 sc1\n"
"v_accvgpr_read_b32 v136, a96\n"
"v_accvgpr_read_b32 v137, a97\n"
"v_accvgpr_read_b32 v138, a98\n"
"v_accvgpr_read_b32 v139, a99\n"
"v_accvgpr_read_b32 v140, a112\n"
"v_accvgpr_read_b32 v141, a113\n"
"v_accvgpr_read_b32 v142, a114\n"
"v_accvgpr_read_b32 v143, a115\n"
"v_mul_f32 v136, v64, v136\n"
"v_mul_f32 v137, v64, v137\n"
"v_mul_f32 v138, v64, v138\n"
"v_mul_f32 v139, v64, v139\n"
"v_mul_f32 v140, v64, v140\n"
"v_mul_f32 v141, v64, v141\n"
"v_mul_f32 v142, v64, v142\n"
"v_mul_f32 v143, v64, v143\n"
"v_cvt_pk_bf16_f32 v156, v136, v137\n"
"v_cvt_pk_bf16_f32 v157, v138, v139\n"
"v_cvt_pk_bf16_f32 v158, v140, v141\n"
"v_cvt_pk_bf16_f32 v159, v142, v143\n"
"buffer_store_dwordx4 v[156:159], v251, s[52:55], s25 offen offset:3072 sc1\n"
"v_mfma_f32_16x16x32_bf16 v[100:103], v[220:223], v[4:7], v[164:167]\n"
"v_exp_f32 v84, v84\n"
"v_mfma_f32_16x16x32_bf16 v[100:103], v[224:227], v[20:23], v[100:103]\n"
"v_exp_f32 v85, v85\n"
"v_mfma_f32_16x16x32_bf16 v[100:103], v[228:231], v[36:39], v[100:103]\n"
"v_exp_f32 v86, v86\n"
"v_mfma_f32_16x16x32_bf16 v[100:103], v[232:235], v[52:55], v[100:103]\n"
"v_exp_f32 v87, v87\n"
"v_mfma_f32_16x16x32_bf16 v[116:119], a[160:163], v[4:7], v[164:167]\n"
"v_cvt_pk_bf16_f32 v186, v84, v85\n"
"v_mfma_f32_16x16x32_bf16 v[116:119], a[164:167], v[20:23], v[116:119]\n"
"v_cvt_pk_bf16_f32 v187, v86, v87\n"
"v_mfma_f32_16x16x32_bf16 v[116:119], a[168:171], v[36:39], v[116:119]\n"
"v_mov_b32 v216, 0\n"
"v_mov_b32 v217, 0\n"
"v_mfma_f32_16x16x32_bf16 v[116:119], a[172:175], v[52:55], v[116:119]\n"
"v_mov_b32 v218, 0\n"
"v_mov_b32 v219, 0\n"
"v_mfma_f32_16x16x32_bf16 a[4:7], a[192:195], v[184:187], a[4:7]\n"
"v_exp_f32 v100, v100\n"
"v_mfma_f32_16x16x32_bf16 a[20:23], a[196:199], v[184:187], a[20:23]\n"
"v_exp_f32 v101, v101\n"
"v_mfma_f32_16x16x32_bf16 a[36:39], a[200:203], v[184:187], a[36:39]\n"
"v_exp_f32 v102, v102\n"
"v_mfma_f32_16x16x32_bf16 a[52:55], a[204:207], v[184:187], a[52:55]\n"
"v_exp_f32 v103, v103\n"
"v_mfma_f32_16x16x32_bf16 a[68:71], a[208:211], v[184:187], a[68:71]\n"
"v_exp_f32 v116, v116\n"
"v_mfma_f32_16x16x32_bf16 a[84:87], a[212:215], v[184:187], a[84:87]\n"
"v_exp_f32 v117, v117\n"
"v_mfma_f32_16x16x32_bf16 a[100:103], a[216:219], v[184:187], a[100:103]\n"
"v_exp_f32 v118, v118\n"
"v_mfma_f32_16x16x32_bf16 a[116:119], a[220:223], v[184:187], a[116:119]\n"
"v_exp_f32 v119, v119\n"
"v_cvt_pk_bf16_f32 v188, v100, v101\n"
"v_cvt_pk_bf16_f32 v189, v102, v103\n"
"v_cvt_pk_bf16_f32 v190, v116, v117\n"
"v_cvt_pk_bf16_f32 v191, v118, v119\n"
"s_nop 1\n"
"v_smfmac_f32_16x16x64_bf16 v[216:219], a[144:147], v[184:191], v239\n"
"v_mfma_f32_16x16x32_bf16 a[4:7], a[224:227], v[188:191], a[4:7]\n"
"v_mfma_f32_16x16x32_bf16 a[20:23], a[228:231], v[188:191], a[20:23]\n"
"v_mfma_f32_16x16x32_bf16 a[36:39], a[232:235], v[188:191], a[36:39]\n"
"v_mfma_f32_16x16x32_bf16 a[52:55], a[236:239], v[188:191], a[52:55]\n"
"v_mfma_f32_16x16x32_bf16 a[68:71], a[240:243], v[188:191], a[68:71]\n"
"v_mfma_f32_16x16x32_bf16 a[84:87], a[244:247], v[188:191], a[84:87]\n"
"s_nop 1\n"
"v_add_f32 v216, v216, v217\n"
"v_add_f32 v218, v218, v219\n"
"v_mfma_f32_16x16x32_bf16 a[100:103], a[248:251], v[188:191], a[100:103]\n"
"v_add_f32 v65, v65, v216\n"
"v_add_f32 v65, v65, v218\n"
"v_mfma_f32_16x16x32_bf16 a[116:119], a[252:255], v[188:191], a[116:119]\n"
"v_add_f32 v69, v69, v65\n"
"v_rcp_f32 v65, v65\n"
"v_accvgpr_read_b32 v128, a4\n"
"v_accvgpr_read_b32 v129, a5\n"
"v_accvgpr_read_b32 v130, a6\n"
"v_accvgpr_read_b32 v131, a7\n"
"v_accvgpr_read_b32 v132, a20\n"
"v_accvgpr_read_b32 v133, a21\n"
"v_accvgpr_read_b32 v134, a22\n"
"v_accvgpr_read_b32 v135, a23\n"
"v_mul_f32 v128, v65, v128\n"
"v_mul_f32 v129, v65, v129\n"
"v_mul_f32 v130, v65, v130\n"
"v_mul_f32 v131, v65, v131\n"
"v_mul_f32 v132, v65, v132\n"
"v_mul_f32 v133, v65, v133\n"
"v_mul_f32 v134, v65, v134\n"
"v_mul_f32 v135, v65, v135\n"
"v_cvt_pk_bf16_f32 v144, v128, v129\n"
"v_cvt_pk_bf16_f32 v145, v130, v131\n"
"v_cvt_pk_bf16_f32 v146, v132, v133\n"
"v_cvt_pk_bf16_f32 v147, v134, v135\n"
"buffer_store_dwordx4 v[144:147], v251, s[52:55], s26 offen sc1\n"
"v_accvgpr_read_b32 v136, a36\n"
"v_accvgpr_read_b32 v137, a37\n"
"v_accvgpr_read_b32 v138, a38\n"
"v_accvgpr_read_b32 v139, a39\n"
"v_accvgpr_read_b32 v140, a52\n"
"v_accvgpr_read_b32 v141, a53\n"
"v_accvgpr_read_b32 v142, a54\n"
"v_accvgpr_read_b32 v143, a55\n"
"v_mul_f32 v136, v65, v136\n"
"v_mul_f32 v137, v65, v137\n"
"v_mul_f32 v138, v65, v138\n"
"v_mul_f32 v139, v65, v139\n"
"v_mul_f32 v140, v65, v140\n"
"v_mul_f32 v141, v65, v141\n"
"v_mul_f32 v142, v65, v142\n"
"v_mul_f32 v143, v65, v143\n"
"v_cvt_pk_bf16_f32 v148, v136, v137\n"
"v_cvt_pk_bf16_f32 v149, v138, v139\n"
"v_cvt_pk_bf16_f32 v150, v140, v141\n"
"v_cvt_pk_bf16_f32 v151, v142, v143\n"
"buffer_store_dwordx4 v[148:151], v251, s[52:55], s26 offen offset:1024 sc1\n"
"v_accvgpr_read_b32 v128, a68\n"
"v_accvgpr_read_b32 v129, a69\n"
"v_accvgpr_read_b32 v130, a70\n"
"v_accvgpr_read_b32 v131, a71\n"
"v_accvgpr_read_b32 v132, a84\n"
"v_accvgpr_read_b32 v133, a85\n"
"v_accvgpr_read_b32 v134, a86\n"
"v_accvgpr_read_b32 v135, a87\n"
"v_mul_f32 v128, v65, v128\n"
"v_mul_f32 v129, v65, v129\n"
"v_mul_f32 v130, v65, v130\n"
"v_mul_f32 v131, v65, v131\n"
"v_mul_f32 v132, v65, v132\n"
"v_mul_f32 v133, v65, v133\n"
"v_mul_f32 v134, v65, v134\n"
"v_mul_f32 v135, v65, v135\n"
"v_cvt_pk_bf16_f32 v152, v128, v129\n"
"v_cvt_pk_bf16_f32 v153, v130, v131\n"
"v_cvt_pk_bf16_f32 v154, v132, v133\n"
"v_cvt_pk_bf16_f32 v155, v134, v135\n"
"buffer_store_dwordx4 v[152:155], v251, s[52:55], s26 offen offset:2048 sc1\n"
"v_accvgpr_read_b32 v136, a100\n"
"v_accvgpr_read_b32 v137, a101\n"
"v_accvgpr_read_b32 v138, a102\n"
"v_accvgpr_read_b32 v139, a103\n"
"v_accvgpr_read_b32 v140, a116\n"
"v_accvgpr_read_b32 v141, a117\n"
"v_accvgpr_read_b32 v142, a118\n"
"v_accvgpr_read_b32 v143, a119\n"
"v_mul_f32 v136, v65, v136\n"
"v_mul_f32 v137, v65, v137\n"
"v_mul_f32 v138, v65, v138\n"
"v_mul_f32 v139, v65, v139\n"
"v_mul_f32 v140, v65, v140\n"
"v_mul_f32 v141, v65, v141\n"
"v_mul_f32 v142, v65, v142\n"
"v_mul_f32 v143, v65, v143\n"
"v_cvt_pk_bf16_f32 v156, v136, v137\n"
"v_cvt_pk_bf16_f32 v157, v138, v139\n"
"v_cvt_pk_bf16_f32 v158, v140, v141\n"
"v_cvt_pk_bf16_f32 v159, v142, v143\n"
"buffer_store_dwordx4 v[156:159], v251, s[52:55], s26 offen offset:3072 sc1\n"
"v_mbcnt_lo_u32_b32 v68, -1, 0\n"
"v_mbcnt_hi_u32_b32 v68, -1, v68\n"
"v_lshlrev_b32 v72, 11, v68\n"
"v_and_b32 v70, 7, v68\n"
"v_lshlrev_b32 v237, 2, v70\n"
"v_cmp_gt_u32 vcc, 8, v68\n"
"v_mov_b32 v70, 0x40000000\n"
"s_nop 0\n"
"v_cndmask_b32 v236, v70, v72, vcc\n"
"s_lshl_b32 s68, s35, 4\n"
"s_lshr_b32 s71, s29, 1\n"
"s_add_u32 s68, s68, s71\n"
"s_lshl_b32 s68, s68, 10\n"
"s_lshr_b32 s71, s34, 2\n"
"s_add_u32 s71, s71, 1\n"
"s_lshl_b32 s71, s71, 7\n"
"s_add_u32 s68, s68, s71\n"
"s_and_b32 s71, s34, 3\n"
"s_lshl_b32 s71, s71, 3\n"
"s_add_u32 s68, s68, s71\n"
"s_and_b32 s71, s29, 1\n"
"s_lshl_b32 s71, s71, 2\n"
"s_add_u32 s24, s68, s71\n"
"v_mfma_f32_16x16x32_bf16 v[104:107], v[220:223], v[8:11], v[168:171]\n"
"v_exp_f32 v88, v88\n"
"v_mfma_f32_16x16x32_bf16 v[104:107], v[224:227], v[24:27], v[104:107]\n"
"v_exp_f32 v89, v89\n"
"v_mfma_f32_16x16x32_bf16 v[104:107], v[228:231], v[40:43], v[104:107]\n"
"v_exp_f32 v90, v90\n"
"v_mfma_f32_16x16x32_bf16 v[104:107], v[232:235], v[56:59], v[104:107]\n"
"v_exp_f32 v91, v91\n"
"v_mfma_f32_16x16x32_bf16 v[120:123], a[160:163], v[8:11], v[168:171]\n"
"v_cvt_pk_bf16_f32 v194, v88, v89\n"
"v_mfma_f32_16x16x32_bf16 v[120:123], a[164:167], v[24:27], v[120:123]\n"
"v_cvt_pk_bf16_f32 v195, v90, v91\n"
"v_mfma_f32_16x16x32_bf16 v[120:123], a[168:171], v[40:43], v[120:123]\n"
"v_mov_b32 v212, 0\n"
"v_mov_b32 v213, 0\n"
"v_mfma_f32_16x16x32_bf16 v[120:123], a[172:175], v[56:59], v[120:123]\n"
"v_mov_b32 v214, 0\n"
"v_mov_b32 v215, 0\n"
"v_mfma_f32_16x16x32_bf16 a[8:11], a[192:195], v[192:195], a[8:11]\n"
"v_exp_f32 v104, v104\n"
"v_mfma_f32_16x16x32_bf16 a[24:27], a[196:199], v[192:195], a[24:27]\n"
"v_exp_f32 v105, v105\n"
"v_mfma_f32_16x16x32_bf16 a[40:43], a[200:203], v[192:195], a[40:43]\n"
"v_exp_f32 v106, v106\n"
"v_mfma_f32_16x16x32_bf16 a[56:59], a[204:207], v[192:195], a[56:59]\n"
"v_exp_f32 v107, v107\n"
"v_mfma_f32_16x16x32_bf16 a[72:75], a[208:211], v[192:195], a[72:75]\n"
"v_exp_f32 v120, v120\n"
"v_mfma_f32_16x16x32_bf16 a[88:91], a[212:215], v[192:195], a[88:91]\n"
"v_exp_f32 v121, v121\n"
"v_mfma_f32_16x16x32_bf16 a[104:107], a[216:219], v[192:195], a[104:107]\n"
"v_exp_f32 v122, v122\n"
"v_mfma_f32_16x16x32_bf16 a[120:123], a[220:223], v[192:195], a[120:123]\n"
"v_exp_f32 v123, v123\n"
"v_cvt_pk_bf16_f32 v196, v104, v105\n"
"v_cvt_pk_bf16_f32 v197, v106, v107\n"
"v_cvt_pk_bf16_f32 v198, v120, v121\n"
"v_cvt_pk_bf16_f32 v199, v122, v123\n"
"s_nop 1\n"
"v_smfmac_f32_16x16x64_bf16 v[212:215], a[144:147], v[192:199], v239\n"
"v_mfma_f32_16x16x32_bf16 a[8:11], a[224:227], v[196:199], a[8:11]\n"
"v_mfma_f32_16x16x32_bf16 a[24:27], a[228:231], v[196:199], a[24:27]\n"
"v_mfma_f32_16x16x32_bf16 a[40:43], a[232:235], v[196:199], a[40:43]\n"
"v_mfma_f32_16x16x32_bf16 a[56:59], a[236:239], v[196:199], a[56:59]\n"
"v_mfma_f32_16x16x32_bf16 a[72:75], a[240:243], v[196:199], a[72:75]\n"
"v_mfma_f32_16x16x32_bf16 a[88:91], a[244:247], v[196:199], a[88:91]\n"
"s_nop 1\n"
"v_add_f32 v212, v212, v213\n"
"v_add_f32 v214, v214, v215\n"
"v_mfma_f32_16x16x32_bf16 a[104:107], a[248:251], v[196:199], a[104:107]\n"
"v_add_f32 v66, v66, v212\n"
"v_add_f32 v66, v66, v214\n"
"v_mfma_f32_16x16x32_bf16 a[120:123], a[252:255], v[196:199], a[120:123]\n"
"s_waitcnt vmcnt(8)\n"
"v_add_f32 v69, v69, v66\n"
"v_rcp_f32 v66, v66\n"
"v_accvgpr_read_b32 v128, a8\n"
"v_accvgpr_read_b32 v129, a9\n"
"v_accvgpr_read_b32 v130, a10\n"
"v_accvgpr_read_b32 v131, a11\n"
"v_accvgpr_read_b32 v132, a24\n"
"v_accvgpr_read_b32 v133, a25\n"
"v_accvgpr_read_b32 v134, a26\n"
"v_accvgpr_read_b32 v135, a27\n"
"v_mul_f32 v128, v66, v128\n"
"v_mul_f32 v129, v66, v129\n"
"v_mul_f32 v130, v66, v130\n"
"v_mul_f32 v131, v66, v131\n"
"v_mul_f32 v132, v66, v132\n"
"v_mul_f32 v133, v66, v133\n"
"v_mul_f32 v134, v66, v134\n"
"v_mul_f32 v135, v66, v135\n"
"v_cvt_pk_bf16_f32 v144, v128, v129\n"
"v_cvt_pk_bf16_f32 v145, v130, v131\n"
"v_cvt_pk_bf16_f32 v146, v132, v133\n"
"v_cvt_pk_bf16_f32 v147, v134, v135\n"
"buffer_store_dwordx4 v[144:147], v251, s[52:55], s22 offen sc1\n"
"v_accvgpr_read_b32 v136, a40\n"
"v_accvgpr_read_b32 v137, a41\n"
"v_accvgpr_read_b32 v138, a42\n"
"v_accvgpr_read_b32 v139, a43\n"
"v_accvgpr_read_b32 v140, a56\n"
"v_accvgpr_read_b32 v141, a57\n"
"v_accvgpr_read_b32 v142, a58\n"
"v_accvgpr_read_b32 v143, a59\n"
"v_mul_f32 v136, v66, v136\n"
"v_mul_f32 v137, v66, v137\n"
"v_mul_f32 v138, v66, v138\n"
"v_mul_f32 v139, v66, v139\n"
"v_mul_f32 v140, v66, v140\n"
"v_mul_f32 v141, v66, v141\n"
"v_mul_f32 v142, v66, v142\n"
"v_mul_f32 v143, v66, v143\n"
"v_cvt_pk_bf16_f32 v148, v136, v137\n"
"v_cvt_pk_bf16_f32 v149, v138, v139\n"
"v_cvt_pk_bf16_f32 v150, v140, v141\n"
"v_cvt_pk_bf16_f32 v151, v142, v143\n"
"buffer_store_dwordx4 v[148:151], v251, s[52:55], s22 offen offset:1024 sc1\n"
"v_accvgpr_read_b32 v128, a72\n"
"v_accvgpr_read_b32 v129, a73\n"
"v_accvgpr_read_b32 v130, a74\n"
"v_accvgpr_read_b32 v131, a75\n"
"v_accvgpr_read_b32 v132, a88\n"
"v_accvgpr_read_b32 v133, a89\n"
"v_accvgpr_read_b32 v134, a90\n"
"v_accvgpr_read_b32 v135, a91\n"
"v_mul_f32 v128, v66, v128\n"
"v_mul_f32 v129, v66, v129\n"
"v_mul_f32 v130, v66, v130\n"
"v_mul_f32 v131, v66, v131\n"
"v_mul_f32 v132, v66, v132\n"
"v_mul_f32 v133, v66, v133\n"
"v_mul_f32 v134, v66, v134\n"
"v_mul_f32 v135, v66, v135\n"
"v_cvt_pk_bf16_f32 v152, v128, v129\n"
"v_cvt_pk_bf16_f32 v153, v130, v131\n"
"v_cvt_pk_bf16_f32 v154, v132, v133\n"
"v_cvt_pk_bf16_f32 v155, v134, v135\n"
"buffer_store_dwordx4 v[152:155], v251, s[52:55], s22 offen offset:2048 sc1\n"
"v_accvgpr_read_b32 v136, a104\n"
"v_accvgpr_read_b32 v137, a105\n"
"v_accvgpr_read_b32 v138, a106\n"
"v_accvgpr_read_b32 v139, a107\n"
"v_accvgpr_read_b32 v140, a120\n"
"v_accvgpr_read_b32 v141, a121\n"
"v_accvgpr_read_b32 v142, a122\n"
"v_accvgpr_read_b32 v143, a123\n"
"v_mul_f32 v136, v66, v136\n"
"v_mul_f32 v137, v66, v137\n"
"v_mul_f32 v138, v66, v138\n"
"v_mul_f32 v139, v66, v139\n"
"v_mul_f32 v140, v66, v140\n"
"v_mul_f32 v141, v66, v141\n"
"v_mul_f32 v142, v66, v142\n"
"v_mul_f32 v143, v66, v143\n"
"v_cvt_pk_bf16_f32 v156, v136, v137\n"
"v_cvt_pk_bf16_f32 v157, v138, v139\n"
"v_cvt_pk_bf16_f32 v158, v140, v141\n"
"v_cvt_pk_bf16_f32 v159, v142, v143\n"
"buffer_store_dwordx4 v[156:159], v251, s[52:55], s22 offen offset:3072 sc1\n"
"v_cmp_ne_u32 vcc, 1, v255\n"
"v_mov_b32 v208, 1\n"
"v_mov_b32 v209, 2\n"
"s_cbranch_vccnz L_hslow_%=\n"
"L_hback_%=:\n"
"buffer_store_dword a156, v253, s[60:63], s74 offen sc1\n"
"v_mfma_f32_16x16x32_bf16 v[108:111], v[220:223], v[12:15], v[172:175]\n"
"v_exp_f32 v92, v92\n"
"v_mfma_f32_16x16x32_bf16 v[108:111], v[224:227], v[28:31], v[108:111]\n"
"v_exp_f32 v93, v93\n"
"v_mfma_f32_16x16x32_bf16 v[108:111], v[228:231], v[44:47], v[108:111]\n"
"v_exp_f32 v94, v94\n"
"v_mfma_f32_16x16x32_bf16 v[108:111], v[232:235], v[60:63], v[108:111]\n"
"v_exp_f32 v95, v95\n"
"v_mfma_f32_16x16x32_bf16 v[124:127], a[160:163], v[12:15], v[172:175]\n"
"v_cvt_pk_bf16_f32 v202, v92, v93\n"
"v_mfma_f32_16x16x32_bf16 v[124:127], a[164:167], v[28:31], v[124:127]\n"
"v_cvt_pk_bf16_f32 v203, v94, v95\n"
"v_mfma_f32_16x16x32_bf16 v[124:127], a[168:171], v[44:47], v[124:127]\n"
"v_mov_b32 v216, 0\n"
"v_mov_b32 v217, 0\n"
"v_mfma_f32_16x16x32_bf16 v[124:127], a[172:175], v[60:63], v[124:127]\n"
"v_mov_b32 v218, 0\n"
"v_mov_b32 v219, 0\n"
"v_mfma_f32_16x16x32_bf16 a[12:15], a[192:195], v[200:203], a[12:15]\n"
"v_exp_f32 v108, v108\n"
"v_mfma_f32_16x16x32_bf16 a[28:31], a[196:199], v[200:203], a[28:31]\n"
"v_exp_f32 v109, v109\n"
"v_mfma_f32_16x16x32_bf16 a[44:47], a[200:203], v[200:203], a[44:47]\n"
"v_exp_f32 v110, v110\n"
"v_mfma_f32_16x16x32_bf16 a[60:63], a[204:207], v[200:203], a[60:63]\n"
"v_exp_f32 v111, v111\n"
"v_mfma_f32_16x16x32_bf16 a[76:79], a[208:211], v[200:203], a[76:79]\n"
"v_exp_f32 v124, v124\n"
"v_mfma_f32_16x16x32_bf16 a[92:95], a[212:215], v[200:203], a[92:95]\n"
"v_exp_f32 v125, v125\n"
"v_mfma_f32_16x16x32_bf16 a[108:111], a[216:219], v[200:203], a[108:111]\n"
"v_exp_f32 v126, v126\n"
"v_mfma_f32_16x16x32_bf16 a[124:127], a[220:223], v[200:203], a[124:127]\n"
"v_exp_f32 v127, v127\n"
"v_cvt_pk_bf16_f32 v204, v108, v109\n"
"v_cvt_pk_bf16_f32 v205, v110, v111\n"
"v_cvt_pk_bf16_f32 v206, v124, v125\n"
"v_cvt_pk_bf16_f32 v207, v126, v127\n"
"s_nop 1\n"
"v_smfmac_f32_16x16x64_bf16 v[216:219], a[144:147], v[200:207], v239\n"
"v_mfma_f32_16x16x32_bf16 a[12:15], a[224:227], v[204:207], a[12:15]\n"
"v_mfma_f32_16x16x32_bf16 a[28:31], a[228:231], v[204:207], a[28:31]\n"
"v_mfma_f32_16x16x32_bf16 a[44:47], a[232:235], v[204:207], a[44:47]\n"
"v_mfma_f32_16x16x32_bf16 a[60:63], a[236:239], v[204:207], a[60:63]\n"
"v_mfma_f32_16x16x32_bf16 a[76:79], a[240:243], v[204:207], a[76:79]\n"
"v_mfma_f32_16x16x32_bf16 a[92:95], a[244:247], v[204:207], a[92:95]\n"
"s_nop 1\n"
"v_add_f32 v216, v216, v217\n"
"v_add_f32 v218, v218, v219\n"
"v_mfma_f32_16x16x32_bf16 a[108:111], a[248:251], v[204:207], a[108:111]\n"
"v_add_f32 v67, v67, v216\n"
"v_add_f32 v67, v67, v218\n"
"v_mfma_f32_16x16x32_bf16 a[124:127], a[252:255], v[204:207], a[124:127]\n"
"v_add_f32 v69, v69, v67\n"
"v_rcp_f32 v67, v67\n"
"v_cmp_ngt_f32 vcc, 0x53800000, v69\n"
"v_accvgpr_read_b32 v128, a12\n"
"v_accvgpr_read_b32 v129, a13\n"
"v_accvgpr_read_b32 v130, a14\n"
"v_accvgpr_read_b32 v131, a15\n"
"v_accvgpr_read_b32 v132, a28\n"
"v_accvgpr_read_b32 v133, a29\n"
"v_accvgpr_read_b32 v134, a30\n"
"v_accvgpr_read_b32 v135, a31\n"
"s_cbranch_vccnz L_slow_%=\n"
"v_mul_f32 v128, v67, v128\n"
"v_mul_f32 v129, v67, v129\n"
"v_mul_f32 v130, v67, v130\n"
"v_mul_f32 v131, v67, v131\n"
"v_mul_f32 v132, v67, v132\n"
"v_mul_f32 v133, v67, v133\n"
"v_mul_f32 v134, v67, v134\n"
"v_mul_f32 v135, v67, v135\n"
"v_cvt_pk_bf16_f32 v144, v128, v129\n"
"v_cvt_pk_bf16_f32 v145, v130, v131\n"
"v_cvt_pk_bf16_f32 v146, v132, v133\n"
"v_cvt_pk_bf16_f32 v147, v134, v135\n"
"s_waitcnt vmcnt(5)\n"
"buffer_store_dword v208, v236, s[60:63], s24 offen sc1\n"
"buffer_store_dwordx4 v[144:147], v251, s[52:55], s23 offen sc1\n"
"v_accvgpr_read_b32 v136, a44\n"
"v_accvgpr_read_b32 v137, a45\n"
"v_accvgpr_read_b32 v138, a46\n"
"v_accvgpr_read_b32 v139, a47\n"
"v_accvgpr_read_b32 v140, a60\n"
"v_accvgpr_read_b32 v141, a61\n"
"v_accvgpr_read_b32 v142, a62\n"
"v_accvgpr_read_b32 v143, a63\n"
"v_mul_f32 v136, v67, v136\n"
"v_mul_f32 v137, v67, v137\n"
"v_mul_f32 v138, v67, v138\n"
"v_mul_f32 v139, v67, v139\n"
"v_mul_f32 v140, v67, v140\n"
"v_mul_f32 v141, v67, v141\n"
"v_mul_f32 v142, v67, v142\n"
"v_mul_f32 v143, v67, v143\n"
"v_cvt_pk_bf16_f32 v148, v136, v137\n"
"v_cvt_pk_bf16_f32 v149, v138, v139\n"
"v_cvt_pk_bf16_f32 v150, v140, v141\n"
"v_cvt_pk_bf16_f32 v151, v142, v143\n"
"buffer_store_dwordx4 v[148:151], v251, s[52:55], s23 offen offset:1024 sc1\n"
"v_accvgpr_read_b32 v128, a76\n"
"v_accvgpr_read_b32 v129, a77\n"
"v_accvgpr_read_b32 v130, a78\n"
"v_accvgpr_read_b32 v131, a79\n"
"v_accvgpr_read_b32 v132, a92\n"
"v_accvgpr_read_b32 v133, a93\n"
"v_accvgpr_read_b32 v134, a94\n"
"v_accvgpr_read_b32 v135, a95\n"
"v_mul_f32 v128, v67, v128\n"
"v_mul_f32 v129, v67, v129\n"
"v_mul_f32 v130, v67, v130\n"
"v_mul_f32 v131, v67, v131\n"
"v_mul_f32 v132, v67, v132\n"
"v_mul_f32 v133, v67, v133\n"
"v_mul_f32 v134, v67, v134\n"
"v_mul_f32 v135, v67, v135\n"
"v_cvt_pk_bf16_f32 v152, v128, v129\n"
"v_cvt_pk_bf16_f32 v153, v130, v131\n"
"v_cvt_pk_bf16_f32 v154, v132, v133\n"
"v_cvt_pk_bf16_f32 v155, v134, v135\n"
"buffer_store_dwordx4 v[152:155], v251, s[52:55], s23 offen offset:2048 sc1\n"
"v_accvgpr_read_b32 v136, a108\n"
"v_accvgpr_read_b32 v137, a109\n"
"v_accvgpr_read_b32 v138, a110\n"
"v_accvgpr_read_b32 v139, a111\n"
"v_accvgpr_read_b32 v140, a124\n"
"v_accvgpr_read_b32 v141, a125\n"
"v_accvgpr_read_b32 v142, a126\n"
"v_accvgpr_read_b32 v143, a127\n"
"v_mul_f32 v136, v67, v136\n"
"v_mul_f32 v137, v67, v137\n"
"v_mul_f32 v138, v67, v138\n"
"v_mul_f32 v139, v67, v139\n"
"v_mul_f32 v140, v67, v140\n"
"v_mul_f32 v141, v67, v141\n"
"v_mul_f32 v142, v67, v142\n"
"v_mul_f32 v143, v67, v143\n"
"v_cvt_pk_bf16_f32 v156, v136, v137\n"
"v_cvt_pk_bf16_f32 v157, v138, v139\n"
"v_cvt_pk_bf16_f32 v158, v140, v141\n"
"v_cvt_pk_bf16_f32 v159, v142, v143\n"
"buffer_store_dwordx4 v[156:159], v251, s[52:55], s23 offen offset:3072 sc1\n"
"L_pub_%=:\n"
"v_add_u32 v80, 0, v249\n"
"v_xor_b32 v81, 32, v80\n"
"v_xor_b32 v82, 64, v80\n"
"v_xor_b32 v83, 96, v80\n"
"v_add_u32 v84, 65536, v249\n"
"v_xor_b32 v85, 32, v84\n"
"v_xor_b32 v86, 64, v84\n"
"v_xor_b32 v87, 96, v84\n"
"v_add_u32 v88, 131072, v249\n"
"v_xor_b32 v89, 32, v88\n"
"v_xor_b32 v90, 64, v88\n"
"v_xor_b32 v91, 96, v88\n"
"ds_read_b64_tr_b16 v[220:221], v84 offset:49152\n"
"ds_read_b64_tr_b16 v[222:223], v84 offset:51200\n"
"ds_read_b64_tr_b16 v[224:225], v85 offset:49152\n"
"ds_read_b64_tr_b16 v[226:227], v85 offset:51200\n"
"ds_read_b64_tr_b16 v[228:229], v86 offset:49152\n"
"ds_read_b64_tr_b16 v[230:231], v86 offset:51200\n"
"ds_read_b64_tr_b16 v[232:233], v87 offset:49152\n"
"ds_read_b64_tr_b16 v[234:235], v87 offset:51200\n"
"ds_read_b64_tr_b16 a[0:1], v88 offset:0\n"
"ds_read_b64_tr_b16 a[2:3], v88 offset:2048\n"
"ds_read_b64_tr_b16 a[4:5], v89 offset:0\n"
"ds_read_b64_tr_b16 a[6:7], v89 offset:2048\n"
"ds_read_b64_tr_b16 a[8:9], v90 offset:0\n"
"ds_read_b64_tr_b16 a[10:11], v90 offset:2048\n"
"ds_read_b64_tr_b16 a[12:13], v91 offset:0\n"
"ds_read_b64_tr_b16 a[14:15], v91 offset:2048\n"
"ds_read_b64_tr_b16 a[16:17], v88 offset:16384\n"
"ds_read_b64_tr_b16 a[18:19], v88 offset:18432\n"
"ds_read_b64_tr_b16 a[20:21], v89 offset:16384\n"
"ds_read_b64_tr_b16 a[22:23], v89 offset:18432\n"
"ds_read_b64_tr_b16 a[24:25], v90 offset:16384\n"
"ds_read_b64_tr_b16 a[26:27], v90 offset:18432\n"
"ds_read_b64_tr_b16 a[28:29], v91 offset:16384\n"
"ds_read_b64_tr_b16 a[30:31], v91 offset:18432\n"
"ds_read_b64_tr_b16 a[32:33], v84 offset:0\n"
"ds_read_b64_tr_b16 a[34:35], v84 offset:2048\n"
"ds_read_b64_tr_b16 a[36:37], v85 offset:0\n"
"ds_read_b64_tr_b16 a[38:39], v85 offset:2048\n"
"ds_read_b64_tr_b16 a[40:41], v86 offset:0\n"
"ds_read_b64_tr_b16 a[42:43], v86 offset:2048\n"
"ds_read_b64_tr_b16 a[44:45], v87 offset:0\n"
"ds_read_b64_tr_b16 a[46:47], v87 offset:2048\n"
"ds_read_b64_tr_b16 a[48:49], v84 offset:32768\n"
"ds_read_b64_tr_b16 a[50:51], v84 offset:34816\n"
"ds_read_b64_tr_b16 a[52:53], v85 offset:32768\n"
"ds_read_b64_tr_b16 a[54:55], v85 offset:34816\n"
"ds_read_b64_tr_b16 a[56:57], v86 offset:32768\n"
"ds_read_b64_tr_b16 a[58:59], v86 offset:34816\n"
"ds_read_b64_tr_b16 a[60:61], v87 offset:32768\n"
"ds_read_b64_tr_b16 a[62:63], v87 offset:34816\n"
"ds_read_b64_tr_b16 a[64:65], v80 offset:32768\n"
"ds_read_b64_tr_b16 a[66:67], v80 offset:34816\n"
"ds_read_b64_tr_b16 a[68:69], v81 offset:32768\n"
"ds_read_b64_tr_b16 a[70:71], v81 offset:34816\n"
"ds_read_b64_tr_b16 a[72:73], v82 offset:32768\n"
"ds_read_b64_tr_b16 a[74:75], v82 offset:34816\n"
"ds_read_b64_tr_b16 a[76:77], v83 offset:32768\n"
"ds_read_b64_tr_b16 a[78:79], v83 offset:34816\n"
"ds_read_b64_tr_b16 a[80:81], v80 offset:49152\n"
"ds_read_b64_tr_b16 a[82:83], v80 offset:51200\n"
"ds_read_b64_tr_b16 a[84:85], v81 offset:49152\n"
"ds_read_b64_tr_b16 a[86:87], v81 offset:51200\n"
"ds_read_b64_tr_b16 a[88:89], v82 offset:49152\n"
"ds_read_b64_tr_b16 a[90:91], v82 offset:51200\n"
"ds_read_b64_tr_b16 a[92:93], v83 offset:49152\n"
"ds_read_b64_tr_b16 a[94:95], v83 offset:51200\n"
"s_waitcnt lgkmcnt(0)\n"
"s_barrier\n"
"v_mbcnt_lo_u32_b32 v68, -1, 0\n"
"v_mbcnt_hi_u32_b32 v68, -1, v68\n"
"s_lshr_b32 s18, s34, 1\n"
"s_and_b32 s18, s18, 0\n"
"s_xor_b32 s18, s18, s35\n"
"s_and_b32 s70, s34, 1\n"
"s_lshl_b32 s70, s70, 15\n"
"s_add_u32 s25, s18, 1\n"
"s_and_b32 s25, s25, 1\n"
"s_lshl_b32 s71, s25, 13\n"
"s_add_u32 s71, s71, s70\n"
"s_xor_b32 s68, s29, 0\n"
"s_and_b32 s72, s68, 1\n"
"s_lshl_b32 s72, s72, 12\n"
"s_lshr_b32 s68, s68, 1\n"
"s_lshl_b32 s68, s68, 14\n"
"s_add_u32 s68, s68, s72\n"
"s_add_u32 s68, s68, s71\n"
"v_add_u32 v176, s68, v251\n"
"s_xor_b32 s68, s29, 1\n"
"s_and_b32 s72, s68, 1\n"
"s_lshl_b32 s72, s72, 12\n"
"s_lshr_b32 s68, s68, 1\n"
"s_lshl_b32 s68, s68, 14\n"
"s_add_u32 s68, s68, s72\n"
"s_add_u32 s68, s68, s71\n"
"v_add_u32 v177, s68, v251\n"
"s_xor_b32 s68, s29, 2\n"
"s_and_b32 s72, s68, 1\n"
"s_lshl_b32 s72, s72, 12\n"
"s_lshr_b32 s68, s68, 1\n"
"s_lshl_b32 s68, s68, 14\n"
"s_add_u32 s68, s68, s72\n"
"s_add_u32 s68, s68, s71\n"
"v_add_u32 v178, s68, v251\n"
"s_xor_b32 s68, s29, 3\n"
"s_and_b32 s72, s68, 1\n"
"s_lshl_b32 s72, s72, 12\n"
"s_lshr_b32 s68, s68, 1\n"
"s_lshl_b32 s68, s68, 14\n"
"s_add_u32 s68, s68, s72\n"
"s_add_u32 s68, s68, s71\n"
"v_add_u32 v179, s68, v251\n"
"s_add_u32 s25, s35, 1\n"
"s_and_b32 s25, s25, 1\n"
"s_lshl_b32 s25, s25, 4\n"
"s_add_u32 s25, s25, s34\n"
"s_lshl_b32 s25, s25, 2\n"
"s_add_u32 s25, s25, s29\n"
"s_lshl_b32 s25, s25, 14\n"
"s_add_u32 s25, s25, 0\n"
"s_add_u32 s26, s18, 2\n"
"s_and_b32 s26, s26, 1\n"
"s_lshl_b32 s71, s26, 13\n"
"s_add_u32 s71, s71, s70\n"
"s_xor_b32 s68, s29, 0\n"
"s_and_b32 s72, s68, 1\n"
"s_lshl_b32 s72, s72, 12\n"
"s_lshr_b32 s68, s68, 1\n"
"s_lshl_b32 s68, s68, 14\n"
"s_add_u32 s68, s68, s72\n"
"s_add_u32 s68, s68, s71\n"
"v_add_u32 v180, s68, v251\n"
"s_xor_b32 s68, s29, 1\n"
"s_and_b32 s72, s68, 1\n"
"s_lshl_b32 s72, s72, 12\n"
"s_lshr_b32 s68, s68, 1\n"
"s_lshl_b32 s68, s68, 14\n"
"s_add_u32 s68, s68, s72\n"
"s_add_u32 s68, s68, s71\n"
"v_add_u32 v181, s68, v251\n"
"s_xor_b32 s68, s29, 2\n"
"s_and_b32 s72, s68, 1\n"
"s_lshl_b32 s72, s72, 12\n"
"s_lshr_b32 s68, s68, 1\n"
"s_lshl_b32 s68, s68, 14\n"
"s_add_u32 s68, s68, s72\n"
"s_add_u32 s68, s68, s71\n"
"v_add_u32 v182, s68, v251\n"
"s_xor_b32 s68, s29, 3\n"
"s_and_b32 s72, s68, 1\n"
"s_lshl_b32 s72, s72, 12\n"
"s_lshr_b32 s68, s68, 1\n"
"s_lshl_b32 s68, s68, 14\n"
"s_add_u32 s68, s68, s72\n"
"s_add_u32 s68, s68, s71\n"
"v_add_u32 v183, s68, v251\n"
"s_xor_b32 s68, s29, 0\n"
"s_mul_i32 s68, s68, 12288\n"
"v_add_u32 v187, s68, v251\n"
"s_xor_b32 s68, s29, 1\n"
"s_mul_i32 s68, s68, 12288\n"
"v_add_u32 v184, s68, v251\n"
"s_xor_b32 s68, s29, 2\n"
"s_mul_i32 s68, s68, 12288\n"
"v_add_u32 v185, s68, v251\n"
"s_xor_b32 s68, s29, 3\n"
"s_mul_i32 s68, s68, 12288\n"
"v_add_u32 v186, s68, v251\n"
"s_lshl_b32 s68, s35, 2\n"
"s_add_u32 s68, s68, s29\n"
"s_lshl_b32 s78, s68, 18\n"
"s_lshr_b32 s68, s80, 1\n"
"s_add_u32 s78, s78, s68\n"
"s_xor_b32 s79, s78, 65536\n"
"s_xor_b32 s30, s78, 131072\n"
"s_xor_b32 s31, s78, 196608\n"
"s_add_u32 s68, s29, 1\n"
"s_lshl_b32 s68, s68, 7\n"
"s_add_u32 s21, s68, s75\n"
"v_mov_b32 v208, 1\n"
"v_mov_b32 v244, 0\n"
"s_lshl_b32 s68, s29, 6\n"
"s_add_u32 s68, s68, 640\n"
"s_add_u32 s22, s68, s75\n"
"s_xor_b32 s26, s22, 16384\n"
"v_lshlrev_b32 v243, 20, v68\n"
"s_lshl_b32 s68, s27, 21\n"
"s_mov_b64 s[40:41], s[16:17]\n"
"s_add_u32 s68, s68, 0x1000000\n"
"s_add_u32 s40, s40, s68\n"
"s_addc_u32 s41, s41, 0\n"
"s_and_b32 s41, s41, 0xffff\n"
"s_mov_b32 s42, 0x200000\n"
"s_mov_b32 s43, 0x20000\n"
"s_lshl_b32 s68, s27, 2\n"
"s_and_b32 s71, s34, 1\n"
"s_lshl_b32 s71, s71, 1\n"
"s_add_u32 s68, s68, s71\n"
"s_lshr_b32 s71, s29, 1\n"
"s_add_u32 s68, s68, s71\n"
"s_lshl_b32 s68, s68, 2\n"
"s_lshl_b32 s71, s18, 1\n"
"s_add_u32 s68, s68, s71\n"
"s_and_b32 s71, s29, 1\n"
"s_add_u32 s68, s68, s71\n"
"s_lshr_b32 s71, s34, 1\n"
"s_lshl_b32 s71, s71, 7\n"
"s_lshl_b32 s68, s68, 14\n"
"s_add_u32 s68, s68, s71\n"
"s_mov_b64 s[36:37], s[14:15]\n"
"s_add_u32 s36, s36, s68\n"
"s_addc_u32 s37, s37, 0\n"
"s_and_b32 s37, s37, 0xffff\n"
"s_mov_b32 s38, 0x4000\n"
"s_mov_b32 s39, 0x20000\n"
"s_sub_u32 s38, s38, s71\n"
"v_and_b32 v70, 15, v68\n"
"v_lshrrev_b32 v72, 4, v68\n"
"v_lshlrev_b32 v72, 4, v72\n"
"v_lshl_add_u32 v240, v70, 10, v72\n"
"s_add_u32 m0, s33, 0\n"
"s_add_u32 s72, s81, 0\n"
"buffer_load_dwordx4 v248, s[56:59], s72 offen lds\n"
"s_add_u32 m0, s33, 1024\n"
"s_add_u32 s72, s81, 8192\n"
"buffer_load_dwordx4 v248, s[56:59], s72 offen lds\n"
"s_add_u32 m0, s33, 2048\n"
"s_add_u32 s72, s81, 16384\n"
"buffer_load_dwordx4 v248, s[56:59], s72 offen lds\n"
"s_add_u32 m0, s33, 3072\n"
"s_add_u32 s72, s81, 24576\n"
"buffer_load_dwordx4 v248, s[56:59], s72 offen lds\n"
"s_add_u32 m0, s33, 16384\n"
"s_add_u32 s72, s81, 32768\n"
"buffer_load_dwordx4 v248, s[56:59], s72 offen lds\n"
"s_add_u32 m0, s33, 17408\n"
"s_add_u32 s72, s81, 40960\n"
"buffer_load_dwordx4 v248, s[56:59], s72 offen lds\n"
"s_add_u32 m0, s33, 18432\n"
"s_add_u32 s72, s81, 49152\n"
"buffer_load_dwordx4 v248, s[56:59], s72 offen lds\n"
"s_add_u32 m0, s33, 19456\n"
"s_add_u32 s72, s81, 57344\n"
"buffer_load_dwordx4 v248, s[56:59], s72 offen lds\n"
"s_waitcnt vmcnt(8)\n"
"buffer_store_dword v209, v236, s[60:63], s24 offen sc1\n"
"s_add_u32 m0, s33, 114688\n"
"s_add_u32 s72, s81, 65536\n"
"buffer_load_dwordx4 v248, s[56:59], s72 offen lds\n"
"s_add_u32 m0, s33, 115712\n"
"s_add_u32 s72, s81, 73728\n"
"buffer_load_dwordx4 v248, s[56:59], s72 offen lds\n"
"s_add_u32 m0, s33, 116736\n"
"s_add_u32 s72, s81, 81920\n"
"buffer_load_dwordx4 v248, s[56:59], s72 offen lds\n"
"s_add_u32 m0, s33, 117760\n"
"s_add_u32 s72, s81, 90112\n"
"buffer_load_dwordx4 v248, s[56:59], s72 offen lds\n"
"s_add_u32 m0, s33, 131072\n"
"s_add_u32 s72, s81, 98304\n"
"buffer_load_dwordx4 v248, s[56:59], s72 offen lds\n"
"s_add_u32 m0, s33, 132096\n"
"s_add_u32 s72, s81, 106496\n"
"buffer_load_dwordx4 v248, s[56:59], s72 offen lds\n"
"s_add_u32 m0, s33, 133120\n"
"s_add_u32 s72, s81, 114688\n"
"buffer_load_dwordx4 v248, s[56:59], s72 offen lds\n"
"s_add_u32 m0, s33, 134144\n"
"s_add_u32 s72, s81, 122880\n"
"buffer_load_dwordx4 v248, s[56:59], s72 offen lds\n"
"s_waitcnt vmcnt(13)\n"
"ds_read_b64_tr_b16 a[96:97], v80 offset:0\n"
"ds_read_b64_tr_b16 a[98:99], v80 offset:2048\n"
"ds_read_b64_tr_b16 a[100:101], v81 offset:0\n"
"ds_read_b64_tr_b16 a[102:103], v81 offset:2048\n"
"ds_read_b64_tr_b16 a[104:105], v82 offset:0\n"
"ds_read_b64_tr_b16 a[106:107], v82 offset:2048\n"
"ds_read_b64_tr_b16 a[108:109], v83 offset:0\n"
"ds_read_b64_tr_b16 a[110:111], v83 offset:2048\n"
"s_add_u32 m0, s33, 147456\n"
"s_add_u32 s72, s81, 131072\n"
"buffer_load_dwordx4 v248, s[56:59], s72 offen lds\n"
"s_add_u32 m0, s33, 148480\n"
"s_add_u32 s72, s81, 139264\n"
"buffer_load_dwordx4 v248, s[56:59], s72 offen lds\n"
"s_add_u32 m0, s33, 149504\n"
"s_add_u32 s72, s81, 147456\n"
"buffer_load_dwordx4 v248, s[56:59], s72 offen lds\n"
"s_add_u32 m0, s33, 150528\n"
"s_add_u32 s72, s81, 155648\n"
"buffer_load_dwordx4 v248, s[56:59], s72 offen lds\n"
"s_waitcnt vmcnt(13)\n"
"ds_read_b64_tr_b16 a[112:113], v80 offset:16384\n"
"ds_read_b64_tr_b16 a[114:115], v80 offset:18432\n"
"ds_read_b64_tr_b16 a[116:117], v81 offset:16384\n"
"ds_read_b64_tr_b16 a[118:119], v81 offset:18432\n"
"ds_read_b64_tr_b16 a[120:121], v82 offset:16384\n"
"ds_read_b64_tr_b16 a[122:123], v82 offset:18432\n"
"ds_read_b64_tr_b16 a[124:125], v83 offset:16384\n"
"ds_read_b64_tr_b16 a[126:127], v83 offset:18432\n"
"s_add_u32 m0, s33, 65536\n"
"s_add_u32 s72, s81, 163840\n"
"buffer_load_dwordx4 v248, s[56:59], s72 offen lds\n"
"s_add_u32 m0, s33, 66560\n"
"s_add_u32 s72, s81, 172032\n"
"buffer_load_dwordx4 v248, s[56:59], s72 offen lds\n"
"s_add_u32 m0, s33, 67584\n"
"s_add_u32 s72, s81, 180224\n"
"buffer_load_dwordx4 v248, s[56:59], s72 offen lds\n"
"s_add_u32 m0, s33, 68608\n"
"s_add_u32 s72, s81, 188416\n"
"buffer_load_dwordx4 v248, s[56:59], s72 offen lds\n"
"s_waitcnt vmcnt(12)\n"
"ds_read_b64_tr_b16 a[128:129], v84 offset:49152\n"
"ds_read_b64_tr_b16 a[130:131], v84 offset:51200\n"
"ds_read_b64_tr_b16 a[132:133], v85 offset:49152\n"
"ds_read_b64_tr_b16 a[134:135], v85 offset:51200\n"
"ds_read_b64_tr_b16 a[136:137], v86 offset:49152\n"
"ds_read_b64_tr_b16 a[138:139], v86 offset:51200\n"
"ds_read_b64_tr_b16 a[140:141], v87 offset:49152\n"
"ds_read_b64_tr_b16 a[142:143], v87 offset:51200\n"
"buffer_load_dword v241, v237, s[60:63], s21 offen sc1\n"
"s_add_u32 m0, s33, 98304\n"
"s_add_u32 s72, s81, 196608\n"
"buffer_load_dwordx4 v248, s[56:59], s72 offen lds\n"
"s_add_u32 m0, s33, 99328\n"
"s_add_u32 s72, s81, 204800\n"
"buffer_load_dwordx4 v248, s[56:59], s72 offen lds\n"
"s_add_u32 m0, s33, 100352\n"
"s_add_u32 s72, s81, 212992\n"
"buffer_load_dwordx4 v248, s[56:59], s72 offen lds\n"
"s_add_u32 m0, s33, 101376\n"
"s_add_u32 s72, s81, 221184\n"
"buffer_load_dwordx4 v248, s[56:59], s72 offen lds\n"
"s_waitcnt vmcnt(13)\n"
"ds_read_b64_tr_b16 a[144:145], v88 offset:0\n"
"ds_read_b64_tr_b16 a[146:147], v88 offset:2048\n"
"ds_read_b64_tr_b16 a[148:149], v89 offset:0\n"
"ds_read_b64_tr_b16 a[150:151], v89 offset:2048\n"
"ds_read_b64_tr_b16 a[152:153], v90 offset:0\n"
"ds_read_b64_tr_b16 a[154:155], v90 offset:2048\n"
"ds_read_b64_tr_b16 a[156:157], v91 offset:0\n"
"ds_read_b64_tr_b16 a[158:159], v91 offset:2048\n"
"s_add_u32 m0, s33, 32768\n"
"s_add_u32 s72, s81, 229376\n"
"buffer_load_dwordx4 v248, s[56:59], s72 offen lds\n"
"s_add_u32 m0, s33, 33792\n"
"s_add_u32 s72, s81, 237568\n"
"buffer_load_dwordx4 v248, s[56:59], s72 offen lds\n"
"s_add_u32 m0, s33, 34816\n"
"s_add_u32 s72, s81, 245760\n"
"buffer_load_dwordx4 v248, s[56:59], s72 offen lds\n"
"s_add_u32 m0, s33, 35840\n"
"s_add_u32 s72, s81, 253952\n"
"buffer_load_dwordx4 v248, s[56:59], s72 offen lds\n"
"s_waitcnt vmcnt(13)\n"
"ds_read_b64_tr_b16 a[160:161], v88 offset:16384\n"
"ds_read_b64_tr_b16 a[162:163], v88 offset:18432\n"
"ds_read_b64_tr_b16 a[164:165], v89 offset:16384\n"
"ds_read_b64_tr_b16 a[166:167], v89 offset:18432\n"
"ds_read_b64_tr_b16 a[168:169], v90 offset:16384\n"
"ds_read_b64_tr_b16 a[170:171], v90 offset:18432\n"
"ds_read_b64_tr_b16 a[172:173], v91 offset:16384\n"
"ds_read_b64_tr_b16 a[174:175], v91 offset:18432\n"
"s_waitcnt vmcnt(8)\n"
"v_cmp_eq_u32 vcc, 0, v241\n"
"s_nop 1\n"
"s_cbranch_vccnz L_oslow_%=\n"
"L_go_%=:\n"
"buffer_load_dwordx4 v[128:131], v177, s[52:55], s78 offen sc1\n"
"buffer_load_dwordx4 v[132:135], v177, s[52:55], s78 offen offset:1024 sc1\n"
"buffer_load_dwordx4 v[136:139], v177, s[52:55], s78 offen offset:2048 sc1\n"
"buffer_load_dwordx4 v[140:143], v177, s[52:55], s78 offen offset:3072 sc1\n"
"buffer_load_dwordx4 v[144:147], v177, s[52:55], s79 offen sc1\n"
"buffer_load_dwordx4 v[148:151], v177, s[52:55], s79 offen offset:1024 sc1\n"
"buffer_load_dwordx4 v[152:155], v177, s[52:55], s79 offen offset:2048 sc1\n"
"buffer_load_dwordx4 v[156:159], v177, s[52:55], s79 offen offset:3072 sc1\n"
"buffer_load_dwordx4 v[160:163], v177, s[52:55], s30 offen sc1\n"
"buffer_load_dwordx4 v[164:167], v177, s[52:55], s30 offen offset:1024 sc1\n"
"ds_read_b64_tr_b16 a[192:193], v84 offset:0\n"
"ds_read_b64_tr_b16 a[194:195], v84 offset:2048\n"
"ds_read_b64_tr_b16 a[196:197], v85 offset:0\n"
"ds_read_b64_tr_b16 a[198:199], v85 offset:2048\n"
"ds_read_b64_tr_b16 a[200:201], v86 offset:0\n"
"ds_read_b64_tr_b16 a[202:203], v86 offset:2048\n"
"ds_read_b64_tr_b16 a[204:205], v87 offset:0\n"
"ds_read_b64_tr_b16 a[206:207], v87 offset:2048\n"
"s_waitcnt vmcnt(14)\n"
"ds_read_b64_tr_b16 a[208:209], v84 offset:32768\n"
"ds_read_b64_tr_b16 a[210:211], v84 offset:34816\n"
"ds_read_b64_tr_b16 a[212:213], v85 offset:32768\n"
"ds_read_b64_tr_b16 a[214:215], v85 offset:34816\n"
"ds_read_b64_tr_b16 a[216:217], v86 offset:32768\n"
"ds_read_b64_tr_b16 a[218:219], v86 offset:34816\n"
"ds_read_b64_tr_b16 a[220:221], v87 offset:32768\n"
"ds_read_b64_tr_b16 a[222:223], v87 offset:34816\n"
"s_waitcnt vmcnt(10)\n"
"ds_read_b64_tr_b16 a[224:225], v80 offset:32768\n"
"ds_read_b64_tr_b16 a[226:227], v80 offset:34816\n"
"ds_read_b64_tr_b16 a[228:229], v81 offset:32768\n"
"ds_read_b64_tr_b16 a[230:231], v81 offset:34816\n"
"ds_read_b64_tr_b16 a[232:233], v82 offset:32768\n"
"ds_read_b64_tr_b16 a[234:235], v82 offset:34816\n"
"ds_read_b64_tr_b16 a[236:237], v83 offset:32768\n"
"ds_read_b64_tr_b16 a[238:239], v83 offset:34816\n"
"s_waitcnt vmcnt(9)\n"
"v_mfma_f32_16x16x32_bf16 v[0:3], a[176:179], v[128:131], 0\n"
"buffer_load_dwordx4 v[168:171], v177, s[52:55], s30 offen offset:2048 sc1\n"
"v_mfma_f32_16x16x32_bf16 v[4:7], a[180:183], v[128:131], 0\n"
"v_mfma_f32_16x16x32_bf16 v[8:11], a[184:187], v[128:131], 0\n"
"buffer_load_dwordx4 v[172:175], v177, s[52:55], s30 offen offset:3072 sc1\n"
"v_mfma_f32_16x16x32_bf16 v[12:15], a[188:191], v[128:131], 0\n"
"s_waitcnt vmcnt(10)\n"
"v_mfma_f32_16x16x32_bf16 v[0:3], v[220:223], v[132:135], v[0:3]\n"
"v_mfma_f32_16x16x32_bf16 v[4:7], v[224:227], v[132:135], v[4:7]\n"
"v_mfma_f32_16x16x32_bf16 v[8:11], v[228:231], v[132:135], v[8:11]\n"
"buffer_load_dwordx4 v[128:131], v177, s[52:55], s31 offen sc1\n"
"v_mfma_f32_16x16x32_bf16 v[12:15], v[232:235], v[132:135], v[12:15]\n"
"s_waitcnt vmcnt(10)\n"
"v_mfma_f32_16x16x32_bf16 v[0:3], a[0:3], v[136:139], v[0:3]\n"
"v_mfma_f32_16x16x32_bf16 v[4:7], a[4:7], v[136:139], v[4:7]\n"
"v_mfma_f32_16x16x32_bf16 v[8:11], a[8:11], v[136:139], v[8:11]\n"
"buffer_load_dwordx4 v[132:135], v177, s[52:55], s31 offen offset:1024 sc1\n"
"v_mfma_f32_16x16x32_bf16 v[12:15], a[12:15], v[136:139], v[12:15]\n"
"s_waitcnt vmcnt(10)\n"
"v_mfma_f32_16x16x32_bf16 v[0:3], a[16:19], v[140:143], v[0:3]\n"
"v_mfma_f32_16x16x32_bf16 v[4:7], a[20:23], v[140:143], v[4:7]\n"
"v_mfma_f32_16x16x32_bf16 v[8:11], a[24:27], v[140:143], v[8:11]\n"
"buffer_load_dwordx4 v[136:139], v177, s[52:55], s31 offen offset:2048 sc1\n"
"v_mfma_f32_16x16x32_bf16 v[12:15], a[28:31], v[140:143], v[12:15]\n"
"s_waitcnt vmcnt(10)\n"
"v_mfma_f32_16x16x32_bf16 v[0:3], a[32:35], v[144:147], v[0:3]\n"
"v_mfma_f32_16x16x32_bf16 v[4:7], a[36:39], v[144:147], v[4:7]\n"
"v_mfma_f32_16x16x32_bf16 v[8:11], a[40:43], v[144:147], v[8:11]\n"
"buffer_load_dwordx4 v[140:143], v177, s[52:55], s31 offen offset:3072 sc1\n"
"v_mfma_f32_16x16x32_bf16 v[12:15], a[44:47], v[144:147], v[12:15]\n"
"s_waitcnt vmcnt(10)\n"
"v_mfma_f32_16x16x32_bf16 v[0:3], a[48:51], v[148:151], v[0:3]\n"
"v_mfma_f32_16x16x32_bf16 v[4:7], a[52:55], v[148:151], v[4:7]\n"
"v_mfma_f32_16x16x32_bf16 v[8:11], a[56:59], v[148:151], v[8:11]\n"
"buffer_load_dwordx4 v[144:147], v178, s[52:55], s78 offen sc1\n"
"v_mfma_f32_16x16x32_bf16 v[12:15], a[60:63], v[148:151], v[12:15]\n"
"s_waitcnt vmcnt(10)\n"
"v_mfma_f32_16x16x32_bf16 v[0:3], a[64:67], v[152:155], v[0:3]\n"
"v_mfma_f32_16x16x32_bf16 v[4:7], a[68:71], v[152:155], v[4:7]\n"
"v_mfma_f32_16x16x32_bf16 v[8:11], a[72:75], v[152:155], v[8:11]\n"
"buffer_load_dwordx4 v[148:151], v178, s[52:55], s78 offen offset:1024 sc1\n"
"v_mfma_f32_16x16x32_bf16 v[12:15], a[76:79], v[152:155], v[12:15]\n"
"s_waitcnt vmcnt(10)\n"
"v_mfma_f32_16x16x32_bf16 v[0:3], a[80:83], v[156:159], v[0:3]\n"
"v_mfma_f32_16x16x32_bf16 v[4:7], a[84:87], v[156:159], v[4:7]\n"
"v_mfma_f32_16x16x32_bf16 v[8:11], a[88:91], v[156:159], v[8:11]\n"
"buffer_load_dwordx4 v[152:155], v178, s[52:55], s78 offen offset:2048 sc1\n"
"v_mfma_f32_16x16x32_bf16 v[12:15], a[92:95], v[156:159], v[12:15]\n"
"s_waitcnt vmcnt(10) lgkmcnt(15)\n"
"v_mfma_f32_16x16x32_bf16 v[0:3], a[96:99], v[160:163], v[0:3]\n"
"v_mfma_f32_16x16x32_bf16 v[4:7], a[100:103], v[160:163], v[4:7]\n"
"v_mfma_f32_16x16x32_bf16 v[8:11], a[104:107], v[160:163], v[8:11]\n"
"buffer_load_dwordx4 v[156:159], v178, s[52:55], s78 offen offset:3072 sc1\n"
"v_mfma_f32_16x16x32_bf16 v[12:15], a[108:111], v[160:163], v[12:15]\n"
"s_waitcnt vmcnt(10)\n"
"v_mfma_f32_16x16x32_bf16 v[0:3], a[112:115], v[164:167], v[0:3]\n"
"v_mfma_f32_16x16x32_bf16 v[4:7], a[116:119], v[164:167], v[4:7]\n"
"v_mfma_f32_16x16x32_bf16 v[8:11], a[120:123], v[164:167], v[8:11]\n"
"buffer_load_dwordx4 v[160:163], v178, s[52:55], s79 offen sc1\n"
"v_mfma_f32_16x16x32_bf16 v[12:15], a[124:127], v[164:167], v[12:15]\n"
"s_waitcnt vmcnt(10)\n"
"v_mfma_f32_16x16x32_bf16 v[0:3], a[128:131], v[168:171], v[0:3]\n"
"v_mfma_f32_16x16x32_bf16 v[4:7], a[132:135], v[168:171], v[4:7]\n"
"v_mfma_f32_16x16x32_bf16 v[8:11], a[136:139], v[168:171], v[8:11]\n"
"buffer_load_dwordx4 v[164:167], v178, s[52:55], s79 offen offset:1024 sc1\n"
"v_mfma_f32_16x16x32_bf16 v[12:15], a[140:143], v[168:171], v[12:15]\n"
"s_waitcnt vmcnt(10)\n"
"v_mfma_f32_16x16x32_bf16 v[0:3], a[144:147], v[172:175], v[0:3]\n"
"v_mfma_f32_16x16x32_bf16 v[4:7], a[148:151], v[172:175], v[4:7]\n"
"v_mfma_f32_16x16x32_bf16 v[8:11], a[152:155], v[172:175], v[8:11]\n"
"buffer_load_dwordx4 v[168:171], v178, s[52:55], s79 offen offset:2048 sc1\n"
"v_mfma_f32_16x16x32_bf16 v[12:15], a[156:159], v[172:175], v[12:15]\n"
"s_waitcnt vmcnt(10)\n"
"v_mfma_f32_16x16x32_bf16 v[0:3], a[160:163], v[128:131], v[0:3]\n"
"v_mfma_f32_16x16x32_bf16 v[4:7], a[164:167], v[128:131], v[4:7]\n"
"v_mfma_f32_16x16x32_bf16 v[8:11], a[168:171], v[128:131], v[8:11]\n"
"buffer_load_dwordx4 v[172:175], v178, s[52:55], s79 offen offset:3072 sc1\n"
"v_mfma_f32_16x16x32_bf16 v[12:15], a[172:175], v[128:131], v[12:15]\n"
"s_waitcnt vmcnt(10)\n"
"v_mfma_f32_16x16x32_bf16 v[0:3], a[192:195], v[132:135], v[0:3]\n"
"v_mfma_f32_16x16x32_bf16 v[4:7], a[196:199], v[132:135], v[4:7]\n"
"v_mfma_f32_16x16x32_bf16 v[8:11], a[200:203], v[132:135], v[8:11]\n"
"buffer_load_dwordx4 v[128:131], v178, s[52:55], s30 offen sc1\n"
"v_mfma_f32_16x16x32_bf16 v[12:15], a[204:207], v[132:135], v[12:15]\n"
"s_waitcnt vmcnt(10) lgkmcnt(14)\n"
"v_mfma_f32_16x16x32_bf16 v[0:3], a[208:211], v[136:139], v[0:3]\n"
"s_waitcnt lgkmcnt(12)\n"
"v_mfma_f32_16x16x32_bf16 v[4:7], a[212:215], v[136:139], v[4:7]\n"
"s_waitcnt lgkmcnt(10)\n"
"v_mfma_f32_16x16x32_bf16 v[8:11], a[216:219], v[136:139], v[8:11]\n"
"s_waitcnt lgkmcnt(8)\n"
"buffer_load_dwordx4 v[132:135], v178, s[52:55], s30 offen offset:1024 sc1\n"
"v_mfma_f32_16x16x32_bf16 v[12:15], a[220:223], v[136:139], v[12:15]\n"
"s_waitcnt vmcnt(10) lgkmcnt(6)\n"
"v_mfma_f32_16x16x32_bf16 v[0:3], a[224:227], v[140:143], v[0:3]\n"
"s_waitcnt lgkmcnt(4)\n"
"v_mfma_f32_16x16x32_bf16 v[4:7], a[228:231], v[140:143], v[4:7]\n"
"s_waitcnt lgkmcnt(2)\n"
"v_mfma_f32_16x16x32_bf16 v[8:11], a[232:235], v[140:143], v[8:11]\n"
"buffer_load_dwordx4 v[136:139], v178, s[52:55], s30 offen offset:2048 sc1\n"
"s_waitcnt lgkmcnt(0)\n"
"s_barrier\n"
"v_mfma_f32_16x16x32_bf16 v[12:15], a[236:239], v[140:143], v[12:15]\n"
"s_waitcnt vmcnt(10)\n"
"v_mfma_f32_16x16x32_bf16 v[16:19], a[176:179], v[144:147], 0\n"
"v_mfma_f32_16x16x32_bf16 v[20:23], a[180:183], v[144:147], 0\n"
"v_mfma_f32_16x16x32_bf16 v[24:27], a[184:187], v[144:147], 0\n"
"buffer_load_dwordx4 v[140:143], v178, s[52:55], s30 offen offset:3072 sc1\n"
"v_mfma_f32_16x16x32_bf16 v[28:31], a[188:191], v[144:147], 0\n"
"s_waitcnt vmcnt(10)\n"
"v_mfma_f32_16x16x32_bf16 v[16:19], v[220:223], v[148:151], v[16:19]\n"
"v_mfma_f32_16x16x32_bf16 v[20:23], v[224:227], v[148:151], v[20:23]\n"
"ds_write_b128 v184, v[0:3] offset:0\n"
"v_mfma_f32_16x16x32_bf16 v[24:27], v[228:231], v[148:151], v[24:27]\n"
"buffer_load_dwordx4 v[144:147], v178, s[52:55], s31 offen sc1\n"
"v_mfma_f32_16x16x32_bf16 v[28:31], v[232:235], v[148:151], v[28:31]\n"
"s_waitcnt vmcnt(10)\n"
"v_mfma_f32_16x16x32_bf16 v[16:19], a[0:3], v[152:155], v[16:19]\n"
"v_mfma_f32_16x16x32_bf16 v[20:23], a[4:7], v[152:155], v[20:23]\n"
"ds_write_b128 v184, v[4:7] offset:1024\n"
"v_mfma_f32_16x16x32_bf16 v[24:27], a[8:11], v[152:155], v[24:27]\n"
"buffer_load_dwordx4 v[148:151], v178, s[52:55], s31 offen offset:1024 sc1\n"
"v_mfma_f32_16x16x32_bf16 v[28:31], a[12:15], v[152:155], v[28:31]\n"
"s_waitcnt vmcnt(10)\n"
"v_mfma_f32_16x16x32_bf16 v[16:19], a[16:19], v[156:159], v[16:19]\n"
"v_mfma_f32_16x16x32_bf16 v[20:23], a[20:23], v[156:159], v[20:23]\n"
"ds_write_b128 v184, v[8:11] offset:2048\n"
"v_mfma_f32_16x16x32_bf16 v[24:27], a[24:27], v[156:159], v[24:27]\n"
"buffer_load_dwordx4 v[152:155], v178, s[52:55], s31 offen offset:2048 sc1\n"
"v_mfma_f32_16x16x32_bf16 v[28:31], a[28:31], v[156:159], v[28:31]\n"
"s_waitcnt vmcnt(10)\n"
"v_mfma_f32_16x16x32_bf16 v[16:19], a[32:35], v[160:163], v[16:19]\n"
"v_mfma_f32_16x16x32_bf16 v[20:23], a[36:39], v[160:163], v[20:23]\n"
"ds_write_b128 v184, v[12:15] offset:3072\n"
"v_mfma_f32_16x16x32_bf16 v[24:27], a[40:43], v[160:163], v[24:27]\n"
"buffer_load_dwordx4 v[156:159], v178, s[52:55], s31 offen offset:3072 sc1\n"
"v_mfma_f32_16x16x32_bf16 v[28:31], a[44:47], v[160:163], v[28:31]\n"
"s_waitcnt vmcnt(10)\n"
"v_mfma_f32_16x16x32_bf16 v[16:19], a[48:51], v[164:167], v[16:19]\n"
"v_mfma_f32_16x16x32_bf16 v[20:23], a[52:55], v[164:167], v[20:23]\n"
"v_mfma_f32_16x16x32_bf16 v[24:27], a[56:59], v[164:167], v[24:27]\n"
"buffer_load_dwordx4 v[160:163], v179, s[52:55], s78 offen sc1\n"
"v_mfma_f32_16x16x32_bf16 v[28:31], a[60:63], v[164:167], v[28:31]\n"
"s_waitcnt vmcnt(10)\n"
"v_mfma_f32_16x16x32_bf16 v[16:19], a[64:67], v[168:171], v[16:19]\n"
"v_mfma_f32_16x16x32_bf16 v[20:23], a[68:71], v[168:171], v[20:23]\n"
"v_mfma_f32_16x16x32_bf16 v[24:27], a[72:75], v[168:171], v[24:27]\n"
"buffer_load_dwordx4 v[164:167], v179, s[52:55], s78 offen offset:1024 sc1\n"
"v_mfma_f32_16x16x32_bf16 v[28:31], a[76:79], v[168:171], v[28:31]\n"
"s_waitcnt vmcnt(10)\n"
"v_mfma_f32_16x16x32_bf16 v[16:19], a[80:83], v[172:175], v[16:19]\n"
"v_mfma_f32_16x16x32_bf16 v[20:23], a[84:87], v[172:175], v[20:23]\n"
"v_mfma_f32_16x16x32_bf16 v[24:27], a[88:91], v[172:175], v[24:27]\n"
"buffer_load_dwordx4 v[168:171], v179, s[52:55], s78 offen offset:2048 sc1\n"
"v_mfma_f32_16x16x32_bf16 v[28:31], a[92:95], v[172:175], v[28:31]\n"
"s_waitcnt vmcnt(10)\n"
"v_mfma_f32_16x16x32_bf16 v[16:19], a[96:99], v[128:131], v[16:19]\n"
"v_mfma_f32_16x16x32_bf16 v[20:23], a[100:103], v[128:131], v[20:23]\n"
"v_mfma_f32_16x16x32_bf16 v[24:27], a[104:107], v[128:131], v[24:27]\n"
"buffer_load_dwordx4 v[172:175], v179, s[52:55], s78 offen offset:3072 sc1\n"
"v_mfma_f32_16x16x32_bf16 v[28:31], a[108:111], v[128:131], v[28:31]\n"
"s_waitcnt vmcnt(10)\n"
"v_mfma_f32_16x16x32_bf16 v[16:19], a[112:115], v[132:135], v[16:19]\n"
"v_mfma_f32_16x16x32_bf16 v[20:23], a[116:119], v[132:135], v[20:23]\n"
"v_mfma_f32_16x16x32_bf16 v[24:27], a[120:123], v[132:135], v[24:27]\n"
"buffer_load_dwordx4 v[128:131], v179, s[52:55], s79 offen sc1\n"
"v_mfma_f32_16x16x32_bf16 v[28:31], a[124:127], v[132:135], v[28:31]\n"
"s_waitcnt vmcnt(10)\n"
"v_mfma_f32_16x16x32_bf16 v[16:19], a[128:131], v[136:139], v[16:19]\n"
"v_mfma_f32_16x16x32_bf16 v[20:23], a[132:135], v[136:139], v[20:23]\n"
"v_mfma_f32_16x16x32_bf16 v[24:27], a[136:139], v[136:139], v[24:27]\n"
"buffer_load_dwordx4 v[132:135], v179, s[52:55], s79 offen offset:1024 sc1\n"
"v_mfma_f32_16x16x32_bf16 v[28:31], a[140:143], v[136:139], v[28:31]\n"
"s_waitcnt vmcnt(10)\n"
"v_mfma_f32_16x16x32_bf16 v[16:19], a[144:147], v[140:143], v[16:19]\n"
"v_mfma_f32_16x16x32_bf16 v[20:23], a[148:151], v[140:143], v[20:23]\n"
"v_mfma_f32_16x16x32_bf16 v[24:27], a[152:155], v[140:143], v[24:27]\n"
"buffer_load_dwordx4 v[136:139], v179, s[52:55], s79 offen offset:2048 sc1\n"
"v_mfma_f32_16x16x32_bf16 v[28:31], a[156:159], v[140:143], v[28:31]\n"
"s_waitcnt vmcnt(10)\n"
"v_mfma_f32_16x16x32_bf16 v[16:19], a[160:163], v[144:147], v[16:19]\n"
"v_mfma_f32_16x16x32_bf16 v[20:23], a[164:167], v[144:147], v[20:23]\n"
"v_mfma_f32_16x16x32_bf16 v[24:27], a[168:171], v[144:147], v[24:27]\n"
"buffer_load_dwordx4 v[140:143], v179, s[52:55], s79 offen offset:3072 sc1\n"
"v_mfma_f32_16x16x32_bf16 v[28:31], a[172:175], v[144:147], v[28:31]\n"
"s_waitcnt vmcnt(10)\n"
"v_mfma_f32_16x16x32_bf16 v[16:19], a[192:195], v[148:151], v[16:19]\n"
"v_mfma_f32_16x16x32_bf16 v[20:23], a[196:199], v[148:151], v[20:23]\n"
"v_mfma_f32_16x16x32_bf16 v[24:27], a[200:203], v[148:151], v[24:27]\n"
"buffer_load_dwordx4 v[144:147], v179, s[52:55], s30 offen sc1\n"
"v_mfma_f32_16x16x32_bf16 v[28:31], a[204:207], v[148:151], v[28:31]\n"
"s_waitcnt vmcnt(10)\n"
"v_mfma_f32_16x16x32_bf16 v[16:19], a[208:211], v[152:155], v[16:19]\n"
"v_mfma_f32_16x16x32_bf16 v[20:23], a[212:215], v[152:155], v[20:23]\n"
"v_mfma_f32_16x16x32_bf16 v[24:27], a[216:219], v[152:155], v[24:27]\n"
"buffer_load_dwordx4 v[148:151], v179, s[52:55], s30 offen offset:1024 sc1\n"
"v_mfma_f32_16x16x32_bf16 v[28:31], a[220:223], v[152:155], v[28:31]\n"
"s_waitcnt vmcnt(10)\n"
"v_mfma_f32_16x16x32_bf16 v[16:19], a[224:227], v[156:159], v[16:19]\n"
"v_mfma_f32_16x16x32_bf16 v[20:23], a[228:231], v[156:159], v[20:23]\n"
"v_mfma_f32_16x16x32_bf16 v[24:27], a[232:235], v[156:159], v[24:27]\n"
"buffer_load_dwordx4 v[152:155], v179, s[52:55], s30 offen offset:2048 sc1\n"
"v_mfma_f32_16x16x32_bf16 v[28:31], a[236:239], v[156:159], v[28:31]\n"
"s_waitcnt vmcnt(10)\n"
"v_mfma_f32_16x16x32_bf16 v[32:35], a[176:179], v[160:163], 0\n"
"v_mfma_f32_16x16x32_bf16 v[36:39], a[180:183], v[160:163], 0\n"
"v_mfma_f32_16x16x32_bf16 v[40:43], a[184:187], v[160:163], 0\n"
"buffer_load_dwordx4 v[156:159], v179, s[52:55], s30 offen offset:3072 sc1\n"
"v_mfma_f32_16x16x32_bf16 v[44:47], a[188:191], v[160:163], 0\n"
"s_waitcnt vmcnt(10)\n"
"v_mfma_f32_16x16x32_bf16 v[32:35], v[220:223], v[164:167], v[32:35]\n"
"v_mfma_f32_16x16x32_bf16 v[36:39], v[224:227], v[164:167], v[36:39]\n"
"ds_write_b128 v185, v[16:19] offset:4096\n"
"v_mfma_f32_16x16x32_bf16 v[40:43], v[228:231], v[164:167], v[40:43]\n"
"buffer_load_dwordx4 v[160:163], v179, s[52:55], s31 offen sc1\n"
"v_mfma_f32_16x16x32_bf16 v[44:47], v[232:235], v[164:167], v[44:47]\n"
"s_waitcnt vmcnt(10)\n"
"v_mfma_f32_16x16x32_bf16 v[32:35], a[0:3], v[168:171], v[32:35]\n"
"v_mfma_f32_16x16x32_bf16 v[36:39], a[4:7], v[168:171], v[36:39]\n"
"ds_write_b128 v185, v[20:23] offset:5120\n"
"v_mfma_f32_16x16x32_bf16 v[40:43], a[8:11], v[168:171], v[40:43]\n"
"buffer_load_dwordx4 v[164:167], v179, s[52:55], s31 offen offset:1024 sc1\n"
"v_mfma_f32_16x16x32_bf16 v[44:47], a[12:15], v[168:171], v[44:47]\n"
"s_waitcnt vmcnt(10)\n"
"v_mfma_f32_16x16x32_bf16 v[32:35], a[16:19], v[172:175], v[32:35]\n"
"v_mfma_f32_16x16x32_bf16 v[36:39], a[20:23], v[172:175], v[36:39]\n"
"ds_write_b128 v185, v[24:27] offset:6144\n"
"v_mfma_f32_16x16x32_bf16 v[40:43], a[24:27], v[172:175], v[40:43]\n"
"buffer_load_dwordx4 v[168:171], v179, s[52:55], s31 offen offset:2048 sc1\n"
"v_mfma_f32_16x16x32_bf16 v[44:47], a[28:31], v[172:175], v[44:47]\n"
"s_waitcnt vmcnt(10)\n"
"v_mfma_f32_16x16x32_bf16 v[32:35], a[32:35], v[128:131], v[32:35]\n"
"v_mfma_f32_16x16x32_bf16 v[36:39], a[36:39], v[128:131], v[36:39]\n"
"ds_write_b128 v185, v[28:31] offset:7168\n"
"v_mfma_f32_16x16x32_bf16 v[40:43], a[40:43], v[128:131], v[40:43]\n"
"buffer_load_dwordx4 v[172:175], v179, s[52:55], s31 offen offset:3072 sc1\n"
"v_mfma_f32_16x16x32_bf16 v[44:47], a[44:47], v[128:131], v[44:47]\n"
"s_waitcnt vmcnt(10)\n"
"v_mfma_f32_16x16x32_bf16 v[32:35], a[48:51], v[132:135], v[32:35]\n"
"v_mfma_f32_16x16x32_bf16 v[36:39], a[52:55], v[132:135], v[36:39]\n"
"v_mfma_f32_16x16x32_bf16 v[40:43], a[56:59], v[132:135], v[40:43]\n"
"buffer_load_dwordx4 v[128:131], v176, s[52:55], s78 offen sc1\n"
"v_mfma_f32_16x16x32_bf16 v[44:47], a[60:63], v[132:135], v[44:47]\n"
"s_waitcnt vmcnt(10)\n"
"v_mfma_f32_16x16x32_bf16 v[32:35], a[64:67], v[136:139], v[32:35]\n"
"v_mfma_f32_16x16x32_bf16 v[36:39], a[68:71], v[136:139], v[36:39]\n"
"v_mfma_f32_16x16x32_bf16 v[40:43], a[72:75], v[136:139], v[40:43]\n"
"buffer_load_dwordx4 v[132:135], v176, s[52:55], s78 offen offset:1024 sc1\n"
"v_mfma_f32_16x16x32_bf16 v[44:47], a[76:79], v[136:139], v[44:47]\n"
"s_waitcnt vmcnt(10)\n"
"v_mfma_f32_16x16x32_bf16 v[32:35], a[80:83], v[140:143], v[32:35]\n"
"buffer_load_dword v241, v237, s[60:63], s21 offen sc1\n"
"v_mfma_f32_16x16x32_bf16 v[36:39], a[84:87], v[140:143], v[36:39]\n"
"v_mfma_f32_16x16x32_bf16 v[40:43], a[88:91], v[140:143], v[40:43]\n"
"buffer_load_dwordx4 v[136:139], v176, s[52:55], s78 offen offset:2048 sc1\n"
"v_mfma_f32_16x16x32_bf16 v[44:47], a[92:95], v[140:143], v[44:47]\n"
"s_waitcnt vmcnt(11)\n"
"v_mfma_f32_16x16x32_bf16 v[32:35], a[96:99], v[144:147], v[32:35]\n"
"v_mfma_f32_16x16x32_bf16 v[36:39], a[100:103], v[144:147], v[36:39]\n"
"v_mfma_f32_16x16x32_bf16 v[40:43], a[104:107], v[144:147], v[40:43]\n"
"buffer_load_dwordx4 v[140:143], v176, s[52:55], s78 offen offset:3072 sc1\n"
"v_mfma_f32_16x16x32_bf16 v[44:47], a[108:111], v[144:147], v[44:47]\n"
"s_waitcnt vmcnt(11)\n"
"v_mfma_f32_16x16x32_bf16 v[32:35], a[112:115], v[148:151], v[32:35]\n"
"v_mfma_f32_16x16x32_bf16 v[36:39], a[116:119], v[148:151], v[36:39]\n"
"v_mfma_f32_16x16x32_bf16 v[40:43], a[120:123], v[148:151], v[40:43]\n"
"buffer_load_dwordx4 v[144:147], v176, s[52:55], s79 offen sc1\n"
"v_mfma_f32_16x16x32_bf16 v[44:47], a[124:127], v[148:151], v[44:47]\n"
"s_waitcnt vmcnt(11)\n"
"v_mfma_f32_16x16x32_bf16 v[32:35], a[128:131], v[152:155], v[32:35]\n"
"v_mfma_f32_16x16x32_bf16 v[36:39], a[132:135], v[152:155], v[36:39]\n"
"v_mfma_f32_16x16x32_bf16 v[40:43], a[136:139], v[152:155], v[40:43]\n"
"buffer_load_dwordx4 v[148:151], v176, s[52:55], s79 offen offset:1024 sc1\n"
"v_mfma_f32_16x16x32_bf16 v[44:47], a[140:143], v[152:155], v[44:47]\n"
"s_waitcnt vmcnt(11)\n"
"v_mfma_f32_16x16x32_bf16 v[32:35], a[144:147], v[156:159], v[32:35]\n"
"v_mfma_f32_16x16x32_bf16 v[36:39], a[148:151], v[156:159], v[36:39]\n"
"v_mfma_f32_16x16x32_bf16 v[40:43], a[152:155], v[156:159], v[40:43]\n"
"buffer_load_dwordx4 v[152:155], v176, s[52:55], s79 offen offset:2048 sc1\n"
"v_mfma_f32_16x16x32_bf16 v[44:47], a[156:159], v[156:159], v[44:47]\n"
"s_waitcnt vmcnt(11)\n"
"v_mfma_f32_16x16x32_bf16 v[32:35], a[160:163], v[160:163], v[32:35]\n"
"v_mfma_f32_16x16x32_bf16 v[36:39], a[164:167], v[160:163], v[36:39]\n"
"v_mfma_f32_16x16x32_bf16 v[40:43], a[168:171], v[160:163], v[40:43]\n"
"buffer_load_dwordx4 v[156:159], v176, s[52:55], s79 offen offset:3072 sc1\n"
"v_mfma_f32_16x16x32_bf16 v[44:47], a[172:175], v[160:163], v[44:47]\n"
"s_waitcnt vmcnt(11)\n"
"v_mfma_f32_16x16x32_bf16 v[32:35], a[192:195], v[164:167], v[32:35]\n"
"v_mfma_f32_16x16x32_bf16 v[36:39], a[196:199], v[164:167], v[36:39]\n"
"v_mfma_f32_16x16x32_bf16 v[40:43], a[200:203], v[164:167], v[40:43]\n"
"buffer_load_dwordx4 v[160:163], v176, s[52:55], s30 offen sc1\n"
"v_mfma_f32_16x16x32_bf16 v[44:47], a[204:207], v[164:167], v[44:47]\n"
"s_waitcnt vmcnt(11)\n"
"v_mfma_f32_16x16x32_bf16 v[32:35], a[208:211], v[168:171], v[32:35]\n"
"v_mfma_f32_16x16x32_bf16 v[36:39], a[212:215], v[168:171], v[36:39]\n"
"v_mfma_f32_16x16x32_bf16 v[40:43], a[216:219], v[168:171], v[40:43]\n"
"buffer_load_dwordx4 v[164:167], v176, s[52:55], s30 offen offset:1024 sc1\n"
"v_mfma_f32_16x16x32_bf16 v[44:47], a[220:223], v[168:171], v[44:47]\n"
"s_waitcnt vmcnt(11)\n"
"v_mfma_f32_16x16x32_bf16 v[32:35], a[224:227], v[172:175], v[32:35]\n"
"v_mfma_f32_16x16x32_bf16 v[36:39], a[228:231], v[172:175], v[36:39]\n"
"v_mfma_f32_16x16x32_bf16 v[40:43], a[232:235], v[172:175], v[40:43]\n"
"buffer_load_dwordx4 v[168:171], v176, s[52:55], s30 offen offset:2048 sc1\n"
"v_mfma_f32_16x16x32_bf16 v[44:47], a[236:239], v[172:175], v[44:47]\n"
"s_waitcnt vmcnt(11)\n"
"v_mfma_f32_16x16x32_bf16 v[0:3], a[176:179], v[128:131], 0\n"
"v_mfma_f32_16x16x32_bf16 v[4:7], a[180:183], v[128:131], 0\n"
"v_mfma_f32_16x16x32_bf16 v[8:11], a[184:187], v[128:131], 0\n"
"buffer_load_dwordx4 v[172:175], v176, s[52:55], s30 offen offset:3072 sc1\n"
"v_mfma_f32_16x16x32_bf16 v[12:15], a[188:191], v[128:131], 0\n"
"s_waitcnt vmcnt(11)\n"
"v_mfma_f32_16x16x32_bf16 v[0:3], v[220:223], v[132:135], v[0:3]\n"
"v_mfma_f32_16x16x32_bf16 v[4:7], v[224:227], v[132:135], v[4:7]\n"
"ds_write_b128 v186, v[32:35] offset:8192\n"
"v_mfma_f32_16x16x32_bf16 v[8:11], v[228:231], v[132:135], v[8:11]\n"
"buffer_load_dwordx4 v[128:131], v176, s[52:55], s31 offen sc1\n"
"v_mfma_f32_16x16x32_bf16 v[12:15], v[232:235], v[132:135], v[12:15]\n"
"s_waitcnt vmcnt(10)\n"
"v_mfma_f32_16x16x32_bf16 v[0:3], a[0:3], v[136:139], v[0:3]\n"
"v_mfma_f32_16x16x32_bf16 v[4:7], a[4:7], v[136:139], v[4:7]\n"
"ds_write_b128 v186, v[36:39] offset:9216\n"
"v_mfma_f32_16x16x32_bf16 v[8:11], a[8:11], v[136:139], v[8:11]\n"
"buffer_load_dwordx4 v[132:135], v176, s[52:55], s31 offen offset:1024 sc1\n"
"v_mfma_f32_16x16x32_bf16 v[12:15], a[12:15], v[136:139], v[12:15]\n"
"s_waitcnt vmcnt(10)\n"
"v_mfma_f32_16x16x32_bf16 v[0:3], a[16:19], v[140:143], v[0:3]\n"
"v_mfma_f32_16x16x32_bf16 v[4:7], a[20:23], v[140:143], v[4:7]\n"
"ds_write_b128 v186, v[40:43] offset:10240\n"
"v_mfma_f32_16x16x32_bf16 v[8:11], a[24:27], v[140:143], v[8:11]\n"
"buffer_load_dwordx4 v[136:139], v176, s[52:55], s31 offen offset:2048 sc1\n"
"v_mfma_f32_16x16x32_bf16 v[12:15], a[28:31], v[140:143], v[12:15]\n"
"s_waitcnt vmcnt(10)\n"
"v_mfma_f32_16x16x32_bf16 v[0:3], a[32:35], v[144:147], v[0:3]\n"
"v_cmp_ne_u32 vcc, 2, v241\n"
"v_mfma_f32_16x16x32_bf16 v[4:7], a[36:39], v[144:147], v[4:7]\n"
"ds_write_b128 v186, v[44:47] offset:11264\n"
"v_mfma_f32_16x16x32_bf16 v[8:11], a[40:43], v[144:147], v[8:11]\n"
"buffer_load_dwordx4 v[140:143], v176, s[52:55], s31 offen offset:3072 sc1\n"
"v_mfma_f32_16x16x32_bf16 v[12:15], a[44:47], v[144:147], v[12:15]\n"
"s_waitcnt vmcnt(10)\n"
"v_mfma_f32_16x16x32_bf16 v[0:3], a[48:51], v[148:151], v[0:3]\n"
"s_cbranch_vccnz L_o2slow_%=\n"
"L_go2_%=:\n"
"v_mfma_f32_16x16x32_bf16 v[4:7], a[52:55], v[148:151], v[4:7]\n"
"v_mfma_f32_16x16x32_bf16 v[8:11], a[56:59], v[148:151], v[8:11]\n"
"buffer_load_dwordx4 v[144:147], v181, s[52:55], s78 offen sc1\n"
"v_mfma_f32_16x16x32_bf16 v[12:15], a[60:63], v[148:151], v[12:15]\n"
"s_waitcnt lgkmcnt(0)\n"
"s_barrier\n"
"s_waitcnt vmcnt(10)\n"
"v_mfma_f32_16x16x32_bf16 v[0:3], a[64:67], v[152:155], v[0:3]\n"
"ds_read_b128 v[48:51], v187 offset:0\n"
"v_mfma_f32_16x16x32_bf16 v[4:7], a[68:71], v[152:155], v[4:7]\n"
"ds_read_b128 v[64:67], v187 offset:4096\n"
"v_mfma_f32_16x16x32_bf16 v[8:11], a[72:75], v[152:155], v[8:11]\n"
"buffer_load_dwordx4 v[148:151], v181, s[52:55], s78 offen offset:1024 sc1\n"
"v_mfma_f32_16x16x32_bf16 v[12:15], a[76:79], v[152:155], v[12:15]\n"
"s_waitcnt vmcnt(10)\n"
"ds_read_b128 v[52:55], v187 offset:1024\n"
"v_mfma_f32_16x16x32_bf16 v[0:3], a[80:83], v[156:159], v[0:3]\n"
"ds_read_b128 v[68:71], v187 offset:5120\n"
"v_mfma_f32_16x16x32_bf16 v[4:7], a[84:87], v[156:159], v[4:7]\n"
"ds_read_b128 v[56:59], v187 offset:2048\n"
"v_mfma_f32_16x16x32_bf16 v[8:11], a[88:91], v[156:159], v[8:11]\n"
"buffer_load_dwordx4 v[152:155], v181, s[52:55], s78 offen offset:2048 sc1\n"
"v_mfma_f32_16x16x32_bf16 v[12:15], a[92:95], v[156:159], v[12:15]\n"
"s_waitcnt vmcnt(10)\n"
"ds_read_b128 v[72:75], v187 offset:6144\n"
"v_mfma_f32_16x16x32_bf16 v[0:3], a[96:99], v[160:163], v[0:3]\n"
"ds_read_b128 v[60:63], v187 offset:3072\n"
"v_mfma_f32_16x16x32_bf16 v[4:7], a[100:103], v[160:163], v[4:7]\n"
"ds_read_b128 v[76:79], v187 offset:7168\n"
"v_mfma_f32_16x16x32_bf16 v[8:11], a[104:107], v[160:163], v[8:11]\n"
"buffer_load_dwordx4 v[156:159], v181, s[52:55], s78 offen offset:3072 sc1\n"
"v_mfma_f32_16x16x32_bf16 v[12:15], a[108:111], v[160:163], v[12:15]\n"
"s_waitcnt vmcnt(10)\n"
"ds_read_b128 v[80:83], v187 offset:8192\n"
"v_mfma_f32_16x16x32_bf16 v[0:3], a[112:115], v[164:167], v[0:3]\n"
"ds_read_b128 v[84:87], v187 offset:9216\n"
"v_mfma_f32_16x16x32_bf16 v[4:7], a[116:119], v[164:167], v[4:7]\n"
"ds_read_b128 v[88:91], v187 offset:10240\n"
"v_mfma_f32_16x16x32_bf16 v[8:11], a[120:123], v[164:167], v[8:11]\n"
"buffer_load_dwordx4 v[160:163], v181, s[52:55], s79 offen sc1\n"
"v_mfma_f32_16x16x32_bf16 v[12:15], a[124:127], v[164:167], v[12:15]\n"
"s_waitcnt vmcnt(10)\n"
"ds_read_b128 v[92:95], v187 offset:11264\n"
"v_mfma_f32_16x16x32_bf16 v[0:3], a[128:131], v[168:171], v[0:3]\n"
"s_waitcnt lgkmcnt(10)\n"
"v_add_f32 v48, v48, v64\n"
"v_add_f32 v49, v49, v65\n"
"v_mfma_f32_16x16x32_bf16 v[4:7], a[132:135], v[168:171], v[4:7]\n"
"v_add_f32 v50, v50, v66\n"
"v_add_f32 v51, v51, v67\n"
"v_mfma_f32_16x16x32_bf16 v[8:11], a[136:139], v[168:171], v[8:11]\n"
"buffer_load_dwordx4 v[164:167], v181, s[52:55], s79 offen offset:1024 sc1\n"
"v_mfma_f32_16x16x32_bf16 v[12:15], a[140:143], v[168:171], v[12:15]\n"
"s_waitcnt vmcnt(10) lgkmcnt(8)\n"
"v_add_f32 v52, v52, v68\n"
"v_add_f32 v53, v53, v69\n"
"v_mfma_f32_16x16x32_bf16 v[0:3], a[144:147], v[172:175], v[0:3]\n"
"v_add_f32 v54, v54, v70\n"
"v_add_f32 v55, v55, v71\n"
"v_mfma_f32_16x16x32_bf16 v[4:7], a[148:151], v[172:175], v[4:7]\n"
"s_waitcnt lgkmcnt(6)\n"
"v_add_f32 v56, v56, v72\n"
"v_add_f32 v57, v57, v73\n"
"v_mfma_f32_16x16x32_bf16 v[8:11], a[152:155], v[172:175], v[8:11]\n"
"buffer_load_dwordx4 v[168:171], v181, s[52:55], s79 offen offset:2048 sc1\n"
"v_mfma_f32_16x16x32_bf16 v[12:15], a[156:159], v[172:175], v[12:15]\n"
"s_waitcnt vmcnt(10)\n"
"v_add_f32 v58, v58, v74\n"
"v_add_f32 v59, v59, v75\n"
"v_mfma_f32_16x16x32_bf16 v[0:3], a[160:163], v[128:131], v[0:3]\n"
"s_waitcnt lgkmcnt(4)\n"
"v_add_f32 v60, v60, v76\n"
"v_add_f32 v61, v61, v77\n"
"v_mfma_f32_16x16x32_bf16 v[4:7], a[164:167], v[128:131], v[4:7]\n"
"v_add_f32 v62, v62, v78\n"
"v_add_f32 v63, v63, v79\n"
"v_mfma_f32_16x16x32_bf16 v[8:11], a[168:171], v[128:131], v[8:11]\n"
"buffer_load_dwordx4 v[172:175], v181, s[52:55], s79 offen offset:3072 sc1\n"
"v_mfma_f32_16x16x32_bf16 v[12:15], a[172:175], v[128:131], v[12:15]\n"
"s_waitcnt vmcnt(10) lgkmcnt(3)\n"
"v_add_f32 v48, v48, v80\n"
"v_add_f32 v49, v49, v81\n"
"v_mfma_f32_16x16x32_bf16 v[0:3], a[192:195], v[132:135], v[0:3]\n"
"v_add_f32 v50, v50, v82\n"
"v_add_f32 v51, v51, v83\n"
"v_mfma_f32_16x16x32_bf16 v[4:7], a[196:199], v[132:135], v[4:7]\n"
"s_waitcnt lgkmcnt(2)\n"
"v_add_f32 v52, v52, v84\n"
"v_add_f32 v53, v53, v85\n"
"v_mfma_f32_16x16x32_bf16 v[8:11], a[200:203], v[132:135], v[8:11]\n"
"buffer_load_dwordx4 v[128:131], v181, s[52:55], s30 offen sc1\n"
"v_mfma_f32_16x16x32_bf16 v[12:15], a[204:207], v[132:135], v[12:15]\n"
"s_waitcnt vmcnt(10)\n"
"v_add_f32 v54, v54, v86\n"
"v_add_f32 v55, v55, v87\n"
"v_mfma_f32_16x16x32_bf16 v[0:3], a[208:211], v[136:139], v[0:3]\n"
"s_waitcnt lgkmcnt(1)\n"
"v_add_f32 v56, v56, v88\n"
"v_add_f32 v57, v57, v89\n"
"v_mfma_f32_16x16x32_bf16 v[4:7], a[212:215], v[136:139], v[4:7]\n"
"v_add_f32 v58, v58, v90\n"
"v_add_f32 v59, v59, v91\n"
"v_mfma_f32_16x16x32_bf16 v[8:11], a[216:219], v[136:139], v[8:11]\n"
"buffer_load_dwordx4 v[132:135], v181, s[52:55], s30 offen offset:1024 sc1\n"
"v_mfma_f32_16x16x32_bf16 v[12:15], a[220:223], v[136:139], v[12:15]\n"
"s_waitcnt vmcnt(10) lgkmcnt(0)\n"
"v_add_f32 v60, v60, v92\n"
"v_add_f32 v61, v61, v93\n"
"v_mfma_f32_16x16x32_bf16 v[0:3], a[224:227], v[140:143], v[0:3]\n"
"v_add_f32 v62, v62, v94\n"
"v_add_f32 v63, v63, v95\n"
"v_mfma_f32_16x16x32_bf16 v[4:7], a[228:231], v[140:143], v[4:7]\n"
"v_mfma_f32_16x16x32_bf16 v[8:11], a[232:235], v[140:143], v[8:11]\n"
"buffer_load_dwordx4 v[136:139], v181, s[52:55], s30 offen offset:2048 sc1\n"
"v_mfma_f32_16x16x32_bf16 v[12:15], a[236:239], v[140:143], v[12:15]\n"
"s_waitcnt vmcnt(10)\n"
"v_mfma_f32_16x16x32_bf16 v[16:19], a[176:179], v[144:147], 0\n"
"v_mfma_f32_16x16x32_bf16 v[20:23], a[180:183], v[144:147], 0\n"
"v_mfma_f32_16x16x32_bf16 v[24:27], a[184:187], v[144:147], 0\n"
"buffer_load_dwordx4 v[140:143], v181, s[52:55], s30 offen offset:3072 sc1\n"
"v_mfma_f32_16x16x32_bf16 v[28:31], a[188:191], v[144:147], 0\n"
"s_waitcnt vmcnt(10)\n"
"v_mfma_f32_16x16x32_bf16 v[16:19], v[220:223], v[148:151], v[16:19]\n"
"v_mfma_f32_16x16x32_bf16 v[20:23], v[224:227], v[148:151], v[20:23]\n"
"v_add_f32 v48, v48, v0\n"
"v_add_f32 v49, v49, v1\n"
"v_mfma_f32_16x16x32_bf16 v[24:27], v[228:231], v[148:151], v[24:27]\n"
"buffer_load_dwordx4 v[144:147], v181, s[52:55], s31 offen sc1\n"
"v_mfma_f32_16x16x32_bf16 v[28:31], v[232:235], v[148:151], v[28:31]\n"
"s_waitcnt vmcnt(10)\n"
"v_add_f32 v50, v50, v2\n"
"v_add_f32 v51, v51, v3\n"
"v_mfma_f32_16x16x32_bf16 v[16:19], a[0:3], v[152:155], v[16:19]\n"
"v_add_f32 v52, v52, v4\n"
"v_add_f32 v53, v53, v5\n"
"v_mfma_f32_16x16x32_bf16 v[20:23], a[4:7], v[152:155], v[20:23]\n"
"v_add_f32 v54, v54, v6\n"
"v_add_f32 v55, v55, v7\n"
"v_mfma_f32_16x16x32_bf16 v[24:27], a[8:11], v[152:155], v[24:27]\n"
"buffer_load_dwordx4 v[148:151], v181, s[52:55], s31 offen offset:1024 sc1\n"
"v_mfma_f32_16x16x32_bf16 v[28:31], a[12:15], v[152:155], v[28:31]\n"
"s_waitcnt vmcnt(10)\n"
"v_add_f32 v56, v56, v8\n"
"v_add_f32 v57, v57, v9\n"
"v_mfma_f32_16x16x32_bf16 v[16:19], a[16:19], v[156:159], v[16:19]\n"
"v_add_f32 v58, v58, v10\n"
"v_add_f32 v59, v59, v11\n"
"v_mfma_f32_16x16x32_bf16 v[20:23], a[20:23], v[156:159], v[20:23]\n"
"v_add_f32 v60, v60, v12\n"
"v_add_f32 v61, v61, v13\n"
"v_mfma_f32_16x16x32_bf16 v[24:27], a[24:27], v[156:159], v[24:27]\n"
"buffer_load_dwordx4 v[152:155], v181, s[52:55], s31 offen offset:2048 sc1\n"
"v_mfma_f32_16x16x32_bf16 v[28:31], a[28:31], v[156:159], v[28:31]\n"
"s_waitcnt vmcnt(10)\n"
"v_add_f32 v62, v62, v14\n"
"v_add_f32 v63, v63, v15\n"
"v_mfma_f32_16x16x32_bf16 v[16:19], a[32:35], v[160:163], v[16:19]\n"
"v_cvt_pk_bf16_f32 v64, v48, v49\n"
"v_cvt_pk_bf16_f32 v65, v50, v51\n"
"v_mfma_f32_16x16x32_bf16 v[20:23], a[36:39], v[160:163], v[20:23]\n"
"v_cvt_pk_bf16_f32 v66, v52, v53\n"
"v_cvt_pk_bf16_f32 v67, v54, v55\n"
"v_mfma_f32_16x16x32_bf16 v[24:27], a[40:43], v[160:163], v[24:27]\n"
"buffer_load_dwordx4 v[156:159], v181, s[52:55], s31 offen offset:3072 sc1\n"
"v_mfma_f32_16x16x32_bf16 v[28:31], a[44:47], v[160:163], v[28:31]\n"
"s_waitcnt vmcnt(10)\n"
"v_cvt_pk_bf16_f32 v68, v56, v57\n"
"v_cvt_pk_bf16_f32 v69, v58, v59\n"
"v_mfma_f32_16x16x32_bf16 v[16:19], a[48:51], v[164:167], v[16:19]\n"
"v_cvt_pk_bf16_f32 v70, v60, v61\n"
"v_cvt_pk_bf16_f32 v71, v62, v63\n"
"v_mfma_f32_16x16x32_bf16 v[20:23], a[52:55], v[164:167], v[20:23]\n"
"buffer_store_dwordx4 v[64:67], v251, s[40:43], s25 offen sc1\n"
"v_mfma_f32_16x16x32_bf16 v[24:27], a[56:59], v[164:167], v[24:27]\n"
"buffer_load_dwordx4 v[160:163], v182, s[52:55], s78 offen sc1\n"
"v_mfma_f32_16x16x32_bf16 v[28:31], a[60:63], v[164:167], v[28:31]\n"
"s_waitcnt vmcnt(11)\n"
"buffer_store_dwordx4 v[68:71], v251, s[40:43], s25 offen offset:1024 sc1\n"
"v_mfma_f32_16x16x32_bf16 v[16:19], a[64:67], v[168:171], v[16:19]\n"
"v_mfma_f32_16x16x32_bf16 v[20:23], a[68:71], v[168:171], v[20:23]\n"
"v_mfma_f32_16x16x32_bf16 v[24:27], a[72:75], v[168:171], v[24:27]\n"
"buffer_load_dwordx4 v[164:167], v182, s[52:55], s78 offen offset:1024 sc1\n"
"v_mfma_f32_16x16x32_bf16 v[28:31], a[76:79], v[168:171], v[28:31]\n"
"s_waitcnt vmcnt(12)\n"
"v_mfma_f32_16x16x32_bf16 v[16:19], a[80:83], v[172:175], v[16:19]\n"
"v_mfma_f32_16x16x32_bf16 v[20:23], a[84:87], v[172:175], v[20:23]\n"
"v_mfma_f32_16x16x32_bf16 v[24:27], a[88:91], v[172:175], v[24:27]\n"
"buffer_load_dwordx4 v[168:171], v182, s[52:55], s78 offen offset:2048 sc1\n"
"v_mfma_f32_16x16x32_bf16 v[28:31], a[92:95], v[172:175], v[28:31]\n"
"s_waitcnt vmcnt(12)\n"
"v_mfma_f32_16x16x32_bf16 v[16:19], a[96:99], v[128:131], v[16:19]\n"
"v_mfma_f32_16x16x32_bf16 v[20:23], a[100:103], v[128:131], v[20:23]\n"
"v_mfma_f32_16x16x32_bf16 v[24:27], a[104:107], v[128:131], v[24:27]\n"
"buffer_load_dwordx4 v[172:175], v182, s[52:55], s78 offen offset:3072 sc1\n"
"v_mfma_f32_16x16x32_bf16 v[28:31], a[108:111], v[128:131], v[28:31]\n"
"s_waitcnt vmcnt(12)\n"
"v_mfma_f32_16x16x32_bf16 v[16:19], a[112:115], v[132:135], v[16:19]\n"
"v_mfma_f32_16x16x32_bf16 v[20:23], a[116:119], v[132:135], v[20:23]\n"
"v_mfma_f32_16x16x32_bf16 v[24:27], a[120:123], v[132:135], v[24:27]\n"
"buffer_load_dwordx4 v[128:131], v182, s[52:55], s79 offen sc1\n"
"v_mfma_f32_16x16x32_bf16 v[28:31], a[124:127], v[132:135], v[28:31]\n"
"s_waitcnt vmcnt(12)\n"
"v_mfma_f32_16x16x32_bf16 v[16:19], a[128:131], v[136:139], v[16:19]\n"
"v_mfma_f32_16x16x32_bf16 v[20:23], a[132:135], v[136:139], v[20:23]\n"
"v_mfma_f32_16x16x32_bf16 v[24:27], a[136:139], v[136:139], v[24:27]\n"
"buffer_load_dwordx4 v[132:135], v182, s[52:55], s79 offen offset:1024 sc1\n"
"v_mfma_f32_16x16x32_bf16 v[28:31], a[140:143], v[136:139], v[28:31]\n"
"s_waitcnt vmcnt(12)\n"
"v_mfma_f32_16x16x32_bf16 v[16:19], a[144:147], v[140:143], v[16:19]\n"
"v_mfma_f32_16x16x32_bf16 v[20:23], a[148:151], v[140:143], v[20:23]\n"
"v_mfma_f32_16x16x32_bf16 v[24:27], a[152:155], v[140:143], v[24:27]\n"
"buffer_load_dwordx4 v[136:139], v182, s[52:55], s79 offen offset:2048 sc1\n"
"v_mfma_f32_16x16x32_bf16 v[28:31], a[156:159], v[140:143], v[28:31]\n"
"s_waitcnt vmcnt(12)\n"
"v_mfma_f32_16x16x32_bf16 v[16:19], a[160:163], v[144:147], v[16:19]\n"
"v_mfma_f32_16x16x32_bf16 v[20:23], a[164:167], v[144:147], v[20:23]\n"
"v_mfma_f32_16x16x32_bf16 v[24:27], a[168:171], v[144:147], v[24:27]\n"
"buffer_load_dwordx4 v[140:143], v182, s[52:55], s79 offen offset:3072 sc1\n"
"v_mfma_f32_16x16x32_bf16 v[28:31], a[172:175], v[144:147], v[28:31]\n"
"s_waitcnt vmcnt(12)\n"
"v_mfma_f32_16x16x32_bf16 v[16:19], a[192:195], v[148:151], v[16:19]\n"
"v_mfma_f32_16x16x32_bf16 v[20:23], a[196:199], v[148:151], v[20:23]\n"
"v_mfma_f32_16x16x32_bf16 v[24:27], a[200:203], v[148:151], v[24:27]\n"
"buffer_load_dwordx4 v[144:147], v182, s[52:55], s30 offen sc1\n"
"v_mfma_f32_16x16x32_bf16 v[28:31], a[204:207], v[148:151], v[28:31]\n"
"s_waitcnt vmcnt(12)\n"
"v_mfma_f32_16x16x32_bf16 v[16:19], a[208:211], v[152:155], v[16:19]\n"
"v_mfma_f32_16x16x32_bf16 v[20:23], a[212:215], v[152:155], v[20:23]\n"
"v_mfma_f32_16x16x32_bf16 v[24:27], a[216:219], v[152:155], v[24:27]\n"
"buffer_load_dwordx4 v[148:151], v182, s[52:55], s30 offen offset:1024 sc1\n"
"v_mfma_f32_16x16x32_bf16 v[28:31], a[220:223], v[152:155], v[28:31]\n"
"s_waitcnt vmcnt(12)\n"
"v_mfma_f32_16x16x32_bf16 v[16:19], a[224:227], v[156:159], v[16:19]\n"
"v_mfma_f32_16x16x32_bf16 v[20:23], a[228:231], v[156:159], v[20:23]\n"
"v_mfma_f32_16x16x32_bf16 v[24:27], a[232:235], v[156:159], v[24:27]\n"
"buffer_load_dwordx4 v[152:155], v182, s[52:55], s30 offen offset:2048 sc1\n"
"v_mfma_f32_16x16x32_bf16 v[28:31], a[236:239], v[156:159], v[28:31]\n"
"s_waitcnt vmcnt(11)\n"
"v_mfma_f32_16x16x32_bf16 v[32:35], a[176:179], v[160:163], 0\n"
"v_mfma_f32_16x16x32_bf16 v[36:39], a[180:183], v[160:163], 0\n"
"v_mfma_f32_16x16x32_bf16 v[40:43], a[184:187], v[160:163], 0\n"
"buffer_load_dwordx4 v[156:159], v182, s[52:55], s30 offen offset:3072 sc1\n"
"v_mfma_f32_16x16x32_bf16 v[44:47], a[188:191], v[160:163], 0\n"
"s_waitcnt vmcnt(10)\n"
"v_mfma_f32_16x16x32_bf16 v[32:35], v[220:223], v[164:167], v[32:35]\n"
"v_mfma_f32_16x16x32_bf16 v[36:39], v[224:227], v[164:167], v[36:39]\n"
"ds_write_b128 v184, v[16:19] offset:49152\n"
"v_mfma_f32_16x16x32_bf16 v[40:43], v[228:231], v[164:167], v[40:43]\n"
"buffer_load_dwordx4 v[160:163], v182, s[52:55], s31 offen sc1\n"
"v_mfma_f32_16x16x32_bf16 v[44:47], v[232:235], v[164:167], v[44:47]\n"
"s_waitcnt vmcnt(10)\n"
"v_mfma_f32_16x16x32_bf16 v[32:35], a[0:3], v[168:171], v[32:35]\n"
"v_mfma_f32_16x16x32_bf16 v[36:39], a[4:7], v[168:171], v[36:39]\n"
"ds_write_b128 v184, v[20:23] offset:50176\n"
"v_mfma_f32_16x16x32_bf16 v[40:43], a[8:11], v[168:171], v[40:43]\n"
"buffer_load_dwordx4 v[164:167], v182, s[52:55], s31 offen offset:1024 sc1\n"
"v_mfma_f32_16x16x32_bf16 v[44:47], a[12:15], v[168:171], v[44:47]\n"
"s_waitcnt vmcnt(10)\n"
"buffer_store_dword v208, v243, s[60:63], s26 offen sc1\n"
"v_mfma_f32_16x16x32_bf16 v[32:35], a[16:19], v[172:175], v[32:35]\n"
"v_mfma_f32_16x16x32_bf16 v[36:39], a[20:23], v[172:175], v[36:39]\n"
"ds_write_b128 v184, v[24:27] offset:51200\n"
"v_mfma_f32_16x16x32_bf16 v[40:43], a[24:27], v[172:175], v[40:43]\n"
"buffer_load_dwordx4 v[168:171], v182, s[52:55], s31 offen offset:2048 sc1\n"
"v_mfma_f32_16x16x32_bf16 v[44:47], a[28:31], v[172:175], v[44:47]\n"
"s_waitcnt vmcnt(11)\n"
"v_mfma_f32_16x16x32_bf16 v[32:35], a[32:35], v[128:131], v[32:35]\n"
"v_mfma_f32_16x16x32_bf16 v[36:39], a[36:39], v[128:131], v[36:39]\n"
"ds_write_b128 v184, v[28:31] offset:52224\n"
"v_mfma_f32_16x16x32_bf16 v[40:43], a[40:43], v[128:131], v[40:43]\n"
"buffer_load_dwordx4 v[172:175], v182, s[52:55], s31 offen offset:3072 sc1\n"
"v_mfma_f32_16x16x32_bf16 v[44:47], a[44:47], v[128:131], v[44:47]\n"
"s_waitcnt vmcnt(11)\n"
"v_mfma_f32_16x16x32_bf16 v[32:35], a[48:51], v[132:135], v[32:35]\n"
"v_mfma_f32_16x16x32_bf16 v[36:39], a[52:55], v[132:135], v[36:39]\n"
"v_mfma_f32_16x16x32_bf16 v[40:43], a[56:59], v[132:135], v[40:43]\n"
"buffer_load_dwordx4 v[128:131], v183, s[52:55], s78 offen sc1\n"
"v_mfma_f32_16x16x32_bf16 v[44:47], a[60:63], v[132:135], v[44:47]\n"
"s_waitcnt vmcnt(11)\n"
"v_mfma_f32_16x16x32_bf16 v[32:35], a[64:67], v[136:139], v[32:35]\n"
"v_mfma_f32_16x16x32_bf16 v[36:39], a[68:71], v[136:139], v[36:39]\n"
"v_mfma_f32_16x16x32_bf16 v[40:43], a[72:75], v[136:139], v[40:43]\n"
"buffer_load_dwordx4 v[132:135], v183, s[52:55], s78 offen offset:1024 sc1\n"
"v_mfma_f32_16x16x32_bf16 v[44:47], a[76:79], v[136:139], v[44:47]\n"
"s_waitcnt vmcnt(11)\n"
"v_mfma_f32_16x16x32_bf16 v[32:35], a[80:83], v[140:143], v[32:35]\n"
"v_mfma_f32_16x16x32_bf16 v[36:39], a[84:87], v[140:143], v[36:39]\n"
"v_mfma_f32_16x16x32_bf16 v[40:43], a[88:91], v[140:143], v[40:43]\n"
"buffer_load_dwordx4 v[136:139], v183, s[52:55], s78 offen offset:2048 sc1\n"
"v_mfma_f32_16x16x32_bf16 v[44:47], a[92:95], v[140:143], v[44:47]\n"
"s_waitcnt vmcnt(11)\n"
"v_mfma_f32_16x16x32_bf16 v[32:35], a[96:99], v[144:147], v[32:35]\n"
"v_mfma_f32_16x16x32_bf16 v[36:39], a[100:103], v[144:147], v[36:39]\n"
"v_mfma_f32_16x16x32_bf16 v[40:43], a[104:107], v[144:147], v[40:43]\n"
"buffer_load_dwordx4 v[140:143], v183, s[52:55], s78 offen offset:3072 sc1\n"
"v_mfma_f32_16x16x32_bf16 v[44:47], a[108:111], v[144:147], v[44:47]\n"
"s_waitcnt vmcnt(11)\n"
"v_mfma_f32_16x16x32_bf16 v[32:35], a[112:115], v[148:151], v[32:35]\n"
"v_mfma_f32_16x16x32_bf16 v[36:39], a[116:119], v[148:151], v[36:39]\n"
"v_mfma_f32_16x16x32_bf16 v[40:43], a[120:123], v[148:151], v[40:43]\n"
"buffer_load_dwordx4 v[144:147], v183, s[52:55], s79 offen sc1\n"
"v_mfma_f32_16x16x32_bf16 v[44:47], a[124:127], v[148:151], v[44:47]\n"
"s_waitcnt vmcnt(11)\n"
"v_mfma_f32_16x16x32_bf16 v[32:35], a[128:131], v[152:155], v[32:35]\n"
"v_mfma_f32_16x16x32_bf16 v[36:39], a[132:135], v[152:155], v[36:39]\n"
"v_mfma_f32_16x16x32_bf16 v[40:43], a[136:139], v[152:155], v[40:43]\n"
"buffer_load_dwordx4 v[148:151], v183, s[52:55], s79 offen offset:1024 sc1\n"
"v_mfma_f32_16x16x32_bf16 v[44:47], a[140:143], v[152:155], v[44:47]\n"
"s_waitcnt vmcnt(11)\n"
"v_mfma_f32_16x16x32_bf16 v[32:35], a[144:147], v[156:159], v[32:35]\n"
"v_mfma_f32_16x16x32_bf16 v[36:39], a[148:151], v[156:159], v[36:39]\n"
"v_mfma_f32_16x16x32_bf16 v[40:43], a[152:155], v[156:159], v[40:43]\n"
"buffer_load_dwordx4 v[152:155], v183, s[52:55], s79 offen offset:2048 sc1\n"
"v_mfma_f32_16x16x32_bf16 v[44:47], a[156:159], v[156:159], v[44:47]\n"
"s_waitcnt vmcnt(11)\n"
"v_mfma_f32_16x16x32_bf16 v[32:35], a[160:163], v[160:163], v[32:35]\n"
"v_mfma_f32_16x16x32_bf16 v[36:39], a[164:167], v[160:163], v[36:39]\n"
"v_mfma_f32_16x16x32_bf16 v[40:43], a[168:171], v[160:163], v[40:43]\n"
"buffer_load_dwordx4 v[156:159], v183, s[52:55], s79 offen offset:3072 sc1\n"
"v_mfma_f32_16x16x32_bf16 v[44:47], a[172:175], v[160:163], v[44:47]\n"
"s_waitcnt vmcnt(11)\n"
"v_mfma_f32_16x16x32_bf16 v[32:35], a[192:195], v[164:167], v[32:35]\n"
"v_mfma_f32_16x16x32_bf16 v[36:39], a[196:199], v[164:167], v[36:39]\n"
"v_mfma_f32_16x16x32_bf16 v[40:43], a[200:203], v[164:167], v[40:43]\n"
"buffer_load_dwordx4 v[160:163], v183, s[52:55], s30 offen sc1\n"
"v_mfma_f32_16x16x32_bf16 v[44:47], a[204:207], v[164:167], v[44:47]\n"
"s_waitcnt vmcnt(10)\n"
"v_mfma_f32_16x16x32_bf16 v[32:35], a[208:211], v[168:171], v[32:35]\n"
"v_mfma_f32_16x16x32_bf16 v[36:39], a[212:215], v[168:171], v[36:39]\n"
"v_mfma_f32_16x16x32_bf16 v[40:43], a[216:219], v[168:171], v[40:43]\n"
"buffer_load_dwordx4 v[164:167], v183, s[52:55], s30 offen offset:1024 sc1\n"
"v_mfma_f32_16x16x32_bf16 v[44:47], a[220:223], v[168:171], v[44:47]\n"
"s_waitcnt vmcnt(10)\n"
"v_mfma_f32_16x16x32_bf16 v[32:35], a[224:227], v[172:175], v[32:35]\n"
"v_mfma_f32_16x16x32_bf16 v[36:39], a[228:231], v[172:175], v[36:39]\n"
"v_mfma_f32_16x16x32_bf16 v[40:43], a[232:235], v[172:175], v[40:43]\n"
"buffer_load_dwordx4 v[168:171], v183, s[52:55], s30 offen offset:2048 sc1\n"
"v_mfma_f32_16x16x32_bf16 v[44:47], a[236:239], v[172:175], v[44:47]\n"
"s_waitcnt vmcnt(10)\n"
"v_mfma_f32_16x16x32_bf16 v[0:3], a[176:179], v[128:131], 0\n"
"v_mfma_f32_16x16x32_bf16 v[4:7], a[180:183], v[128:131], 0\n"
"v_mfma_f32_16x16x32_bf16 v[8:11], a[184:187], v[128:131], 0\n"
"buffer_load_dwordx4 v[172:175], v183, s[52:55], s30 offen offset:3072 sc1\n"
"v_mfma_f32_16x16x32_bf16 v[12:15], a[188:191], v[128:131], 0\n"
"s_waitcnt vmcnt(10)\n"
"v_mfma_f32_16x16x32_bf16 v[0:3], v[220:223], v[132:135], v[0:3]\n"
"v_mfma_f32_16x16x32_bf16 v[4:7], v[224:227], v[132:135], v[4:7]\n"
"ds_write_b128 v185, v[32:35] offset:53248\n"
"v_mfma_f32_16x16x32_bf16 v[8:11], v[228:231], v[132:135], v[8:11]\n"
"buffer_load_dwordx4 v[128:131], v183, s[52:55], s31 offen sc1\n"
"v_mfma_f32_16x16x32_bf16 v[12:15], v[232:235], v[132:135], v[12:15]\n"
"s_waitcnt vmcnt(10)\n"
"v_mfma_f32_16x16x32_bf16 v[0:3], a[0:3], v[136:139], v[0:3]\n"
"v_mfma_f32_16x16x32_bf16 v[4:7], a[4:7], v[136:139], v[4:7]\n"
"ds_write_b128 v185, v[36:39] offset:54272\n"
"v_mfma_f32_16x16x32_bf16 v[8:11], a[8:11], v[136:139], v[8:11]\n"
"buffer_load_dwordx4 v[132:135], v183, s[52:55], s31 offen offset:1024 sc1\n"
"v_mfma_f32_16x16x32_bf16 v[12:15], a[12:15], v[136:139], v[12:15]\n"
"s_waitcnt vmcnt(10)\n"
"v_mfma_f32_16x16x32_bf16 v[0:3], a[16:19], v[140:143], v[0:3]\n"
"v_mfma_f32_16x16x32_bf16 v[4:7], a[20:23], v[140:143], v[4:7]\n"
"ds_write_b128 v185, v[40:43] offset:55296\n"
"v_mfma_f32_16x16x32_bf16 v[8:11], a[24:27], v[140:143], v[8:11]\n"
"buffer_load_dwordx4 v[136:139], v183, s[52:55], s31 offen offset:2048 sc1\n"
"v_mfma_f32_16x16x32_bf16 v[12:15], a[28:31], v[140:143], v[12:15]\n"
"s_waitcnt vmcnt(10)\n"
"buffer_load_dword v241, v244, s[60:63], s22 offen sc1\n"
"v_mfma_f32_16x16x32_bf16 v[0:3], a[32:35], v[144:147], v[0:3]\n"
"v_mfma_f32_16x16x32_bf16 v[4:7], a[36:39], v[144:147], v[4:7]\n"
"ds_write_b128 v185, v[44:47] offset:56320\n"
"v_mfma_f32_16x16x32_bf16 v[8:11], a[40:43], v[144:147], v[8:11]\n"
"buffer_load_dwordx4 v[140:143], v183, s[52:55], s31 offen offset:3072 sc1\n"
"v_mfma_f32_16x16x32_bf16 v[12:15], a[44:47], v[144:147], v[12:15]\n"
"s_waitcnt vmcnt(11)\n"
"v_mfma_f32_16x16x32_bf16 v[0:3], a[48:51], v[148:151], v[0:3]\n"
"v_mfma_f32_16x16x32_bf16 v[4:7], a[52:55], v[148:151], v[4:7]\n"
"v_mfma_f32_16x16x32_bf16 v[8:11], a[56:59], v[148:151], v[8:11]\n"
"buffer_load_dwordx4 v[144:147], v180, s[52:55], s78 offen sc1\n"
"v_mfma_f32_16x16x32_bf16 v[12:15], a[60:63], v[148:151], v[12:15]\n"
"s_waitcnt vmcnt(11)\n"
"v_mfma_f32_16x16x32_bf16 v[0:3], a[64:67], v[152:155], v[0:3]\n"
"v_mfma_f32_16x16x32_bf16 v[4:7], a[68:71], v[152:155], v[4:7]\n"
"v_mfma_f32_16x16x32_bf16 v[8:11], a[72:75], v[152:155], v[8:11]\n"
"buffer_load_dwordx4 v[148:151], v180, s[52:55], s78 offen offset:1024 sc1\n"
"v_mfma_f32_16x16x32_bf16 v[12:15], a[76:79], v[152:155], v[12:15]\n"
"s_waitcnt vmcnt(11)\n"
"v_mfma_f32_16x16x32_bf16 v[0:3], a[80:83], v[156:159], v[0:3]\n"
"v_mfma_f32_16x16x32_bf16 v[4:7], a[84:87], v[156:159], v[4:7]\n"
"v_mfma_f32_16x16x32_bf16 v[8:11], a[88:91], v[156:159], v[8:11]\n"
"buffer_load_dwordx4 v[152:155], v180, s[52:55], s78 offen offset:2048 sc1\n"
"v_mfma_f32_16x16x32_bf16 v[12:15], a[92:95], v[156:159], v[12:15]\n"
"s_waitcnt vmcnt(11)\n"
"v_mfma_f32_16x16x32_bf16 v[0:3], a[96:99], v[160:163], v[0:3]\n"
"v_mfma_f32_16x16x32_bf16 v[4:7], a[100:103], v[160:163], v[4:7]\n"
"v_mfma_f32_16x16x32_bf16 v[8:11], a[104:107], v[160:163], v[8:11]\n"
"buffer_load_dwordx4 v[156:159], v180, s[52:55], s78 offen offset:3072 sc1\n"
"v_mfma_f32_16x16x32_bf16 v[12:15], a[108:111], v[160:163], v[12:15]\n"
"s_waitcnt vmcnt(11)\n"
"v_mfma_f32_16x16x32_bf16 v[0:3], a[112:115], v[164:167], v[0:3]\n"
"v_mfma_f32_16x16x32_bf16 v[4:7], a[116:119], v[164:167], v[4:7]\n"
"v_mfma_f32_16x16x32_bf16 v[8:11], a[120:123], v[164:167], v[8:11]\n"
"buffer_load_dwordx4 v[160:163], v180, s[52:55], s79 offen sc1\n"
"v_mfma_f32_16x16x32_bf16 v[12:15], a[124:127], v[164:167], v[12:15]\n"
"s_waitcnt vmcnt(11)\n"
"v_mfma_f32_16x16x32_bf16 v[0:3], a[128:131], v[168:171], v[0:3]\n"
"v_mfma_f32_16x16x32_bf16 v[4:7], a[132:135], v[168:171], v[4:7]\n"
"v_mfma_f32_16x16x32_bf16 v[8:11], a[136:139], v[168:171], v[8:11]\n"
"buffer_load_dwordx4 v[164:167], v180, s[52:55], s79 offen offset:1024 sc1\n"
"v_mfma_f32_16x16x32_bf16 v[12:15], a[140:143], v[168:171], v[12:15]\n"
"s_waitcnt vmcnt(11)\n"
"v_mfma_f32_16x16x32_bf16 v[0:3], a[144:147], v[172:175], v[0:3]\n"
"v_mfma_f32_16x16x32_bf16 v[4:7], a[148:151], v[172:175], v[4:7]\n"
"v_mfma_f32_16x16x32_bf16 v[8:11], a[152:155], v[172:175], v[8:11]\n"
"buffer_load_dwordx4 v[168:171], v180, s[52:55], s79 offen offset:2048 sc1\n"
"v_mfma_f32_16x16x32_bf16 v[12:15], a[156:159], v[172:175], v[12:15]\n"
"s_waitcnt vmcnt(11)\n"
"v_mfma_f32_16x16x32_bf16 v[0:3], a[160:163], v[128:131], v[0:3]\n"
"v_mfma_f32_16x16x32_bf16 v[4:7], a[164:167], v[128:131], v[4:7]\n"
"v_mfma_f32_16x16x32_bf16 v[8:11], a[168:171], v[128:131], v[8:11]\n"
"buffer_load_dwordx4 v[172:175], v180, s[52:55], s79 offen offset:3072 sc1\n"
"v_mfma_f32_16x16x32_bf16 v[12:15], a[172:175], v[128:131], v[12:15]\n"
"s_waitcnt vmcnt(11)\n"
"v_mfma_f32_16x16x32_bf16 v[0:3], a[192:195], v[132:135], v[0:3]\n"
"v_mfma_f32_16x16x32_bf16 v[4:7], a[196:199], v[132:135], v[4:7]\n"
"v_mfma_f32_16x16x32_bf16 v[8:11], a[200:203], v[132:135], v[8:11]\n"
"buffer_load_dwordx4 v[128:131], v180, s[52:55], s30 offen sc1\n"
"v_mfma_f32_16x16x32_bf16 v[12:15], a[204:207], v[132:135], v[12:15]\n"
"s_waitcnt vmcnt(11)\n"
"v_mfma_f32_16x16x32_bf16 v[0:3], a[208:211], v[136:139], v[0:3]\n"
"v_mfma_f32_16x16x32_bf16 v[4:7], a[212:215], v[136:139], v[4:7]\n"
"v_mfma_f32_16x16x32_bf16 v[8:11], a[216:219], v[136:139], v[8:11]\n"
"buffer_load_dwordx4 v[132:135], v180, s[52:55], s30 offen offset:1024 sc1\n"
"v_mfma_f32_16x16x32_bf16 v[12:15], a[220:223], v[136:139], v[12:15]\n"
"s_waitcnt vmcnt(10)\n"
"v_mfma_f32_16x16x32_bf16 v[0:3], a[224:227], v[140:143], v[0:3]\n"
"v_mfma_f32_16x16x32_bf16 v[4:7], a[228:231], v[140:143], v[4:7]\n"
"v_mfma_f32_16x16x32_bf16 v[8:11], a[232:235], v[140:143], v[8:11]\n"
"buffer_load_dwordx4 v[136:139], v180, s[52:55], s30 offen offset:2048 sc1\n"
"v_mfma_f32_16x16x32_bf16 v[12:15], a[236:239], v[140:143], v[12:15]\n"
"s_waitcnt vmcnt(10)\n"
"v_cmp_ne_u32 vcc, 1, v241\n"
"v_mfma_f32_16x16x32_bf16 v[16:19], a[176:179], v[144:147], 0\n"
"v_mfma_f32_16x16x32_bf16 v[20:23], a[180:183], v[144:147], 0\n"
"s_cbranch_vccnz L_xslow_%=\n"
"L_xback_%=:\n"
"v_mfma_f32_16x16x32_bf16 v[24:27], a[184:187], v[144:147], 0\n"
"buffer_load_dwordx4 v[140:143], v180, s[52:55], s30 offen offset:3072 sc1\n"
"v_mfma_f32_16x16x32_bf16 v[28:31], a[188:191], v[144:147], 0\n"
"s_waitcnt vmcnt(10)\n"
"buffer_load_dwordx4 v[112:115], v251, s[64:67], 0 offen sc1\n"
"v_mfma_f32_16x16x32_bf16 v[16:19], v[220:223], v[148:151], v[16:19]\n"
"v_mfma_f32_16x16x32_bf16 v[20:23], v[224:227], v[148:151], v[20:23]\n"
"ds_write_b128 v186, v[0:3] offset:57344\n"
"buffer_load_dwordx4 v[116:119], v251, s[64:67], 0 offen offset:1024 sc1\n"
"v_mfma_f32_16x16x32_bf16 v[24:27], v[228:231], v[148:151], v[24:27]\n"
"buffer_load_dwordx4 v[144:147], v180, s[52:55], s31 offen sc1\n"
"v_mfma_f32_16x16x32_bf16 v[28:31], v[232:235], v[148:151], v[28:31]\n"
"s_waitcnt vmcnt(12)\n"
"v_mfma_f32_16x16x32_bf16 v[16:19], a[0:3], v[152:155], v[16:19]\n"
"v_mfma_f32_16x16x32_bf16 v[20:23], a[4:7], v[152:155], v[20:23]\n"
"ds_write_b128 v186, v[4:7] offset:58368\n"
"v_mfma_f32_16x16x32_bf16 v[24:27], a[8:11], v[152:155], v[24:27]\n"
"buffer_load_dwordx4 v[148:151], v180, s[52:55], s31 offen offset:1024 sc1\n"
"v_mfma_f32_16x16x32_bf16 v[28:31], a[12:15], v[152:155], v[28:31]\n"
"s_waitcnt vmcnt(12)\n"
"v_mfma_f32_16x16x32_bf16 v[16:19], a[16:19], v[156:159], v[16:19]\n"
"v_mfma_f32_16x16x32_bf16 v[20:23], a[20:23], v[156:159], v[20:23]\n"
"ds_write_b128 v186, v[8:11] offset:59392\n"
"v_mfma_f32_16x16x32_bf16 v[24:27], a[24:27], v[156:159], v[24:27]\n"
"buffer_load_dwordx4 v[152:155], v180, s[52:55], s31 offen offset:2048 sc1\n"
"v_mfma_f32_16x16x32_bf16 v[28:31], a[28:31], v[156:159], v[28:31]\n"
"s_waitcnt vmcnt(12)\n"
"v_mfma_f32_16x16x32_bf16 v[16:19], a[32:35], v[160:163], v[16:19]\n"
"v_mfma_f32_16x16x32_bf16 v[20:23], a[36:39], v[160:163], v[20:23]\n"
"ds_write_b128 v186, v[12:15] offset:60416\n"
"v_mfma_f32_16x16x32_bf16 v[24:27], a[40:43], v[160:163], v[24:27]\n"
"buffer_load_dwordx4 v[156:159], v180, s[52:55], s31 offen offset:3072 sc1\n"
"v_mfma_f32_16x16x32_bf16 v[28:31], a[44:47], v[160:163], v[28:31]\n"
"s_waitcnt vmcnt(12)\n"
"v_mfma_f32_16x16x32_bf16 v[16:19], a[48:51], v[164:167], v[16:19]\n"
"v_mfma_f32_16x16x32_bf16 v[20:23], a[52:55], v[164:167], v[20:23]\n"
"v_mfma_f32_16x16x32_bf16 v[24:27], a[56:59], v[164:167], v[24:27]\n"
"v_mfma_f32_16x16x32_bf16 v[28:31], a[60:63], v[164:167], v[28:31]\n"
"s_waitcnt lgkmcnt(0)\n"
"s_barrier\n"
"s_waitcnt vmcnt(11)\n"
"v_mfma_f32_16x16x32_bf16 v[16:19], a[64:67], v[168:171], v[16:19]\n"
"ds_read_b128 v[48:51], v187 offset:49152\n"
"v_mfma_f32_16x16x32_bf16 v[20:23], a[68:71], v[168:171], v[20:23]\n"
"ds_read_b128 v[64:67], v187 offset:53248\n"
"v_mfma_f32_16x16x32_bf16 v[24:27], a[72:75], v[168:171], v[24:27]\n"
"ds_read_b128 v[52:55], v187 offset:50176\n"
"v_mfma_f32_16x16x32_bf16 v[28:31], a[76:79], v[168:171], v[28:31]\n"
"s_waitcnt vmcnt(10)\n"
"ds_read_b128 v[68:71], v187 offset:54272\n"
"v_mfma_f32_16x16x32_bf16 v[16:19], a[80:83], v[172:175], v[16:19]\n"
"ds_read_b128 v[56:59], v187 offset:51200\n"
"v_mfma_f32_16x16x32_bf16 v[20:23], a[84:87], v[172:175], v[20:23]\n"
"ds_read_b128 v[72:75], v187 offset:55296\n"
"v_mfma_f32_16x16x32_bf16 v[24:27], a[88:91], v[172:175], v[24:27]\n"
"ds_read_b128 v[60:63], v187 offset:52224\n"
"v_mfma_f32_16x16x32_bf16 v[28:31], a[92:95], v[172:175], v[28:31]\n"
"s_waitcnt vmcnt(9)\n"
"ds_read_b128 v[76:79], v187 offset:56320\n"
"v_mfma_f32_16x16x32_bf16 v[16:19], a[96:99], v[128:131], v[16:19]\n"
"ds_read_b128 v[80:83], v187 offset:57344\n"
"v_mfma_f32_16x16x32_bf16 v[20:23], a[100:103], v[128:131], v[20:23]\n"
"ds_read_b128 v[84:87], v187 offset:58368\n"
"v_mfma_f32_16x16x32_bf16 v[24:27], a[104:107], v[128:131], v[24:27]\n"
"ds_read_b128 v[88:91], v187 offset:59392\n"
"v_mfma_f32_16x16x32_bf16 v[28:31], a[108:111], v[128:131], v[28:31]\n"
"s_waitcnt vmcnt(8)\n"
"ds_read_b128 v[92:95], v187 offset:60416\n"
"v_mfma_f32_16x16x32_bf16 v[16:19], a[112:115], v[132:135], v[16:19]\n"
"s_waitcnt lgkmcnt(10)\n"
"v_add_f32 v48, v48, v64\n"
"v_add_f32 v49, v49, v65\n"
"v_mfma_f32_16x16x32_bf16 v[20:23], a[116:119], v[132:135], v[20:23]\n"
"v_add_f32 v50, v50, v66\n"
"v_add_f32 v51, v51, v67\n"
"v_mfma_f32_16x16x32_bf16 v[24:27], a[120:123], v[132:135], v[24:27]\n"
"s_waitcnt lgkmcnt(8)\n"
"v_add_f32 v52, v52, v68\n"
"v_add_f32 v53, v53, v69\n"
"v_mfma_f32_16x16x32_bf16 v[28:31], a[124:127], v[132:135], v[28:31]\n"
"s_waitcnt vmcnt(7)\n"
"v_add_f32 v54, v54, v70\n"
"v_add_f32 v55, v55, v71\n"
"v_mfma_f32_16x16x32_bf16 v[16:19], a[128:131], v[136:139], v[16:19]\n"
"s_waitcnt lgkmcnt(6)\n"
"v_add_f32 v56, v56, v72\n"
"v_add_f32 v57, v57, v73\n"
"v_mfma_f32_16x16x32_bf16 v[20:23], a[132:135], v[136:139], v[20:23]\n"
"v_add_f32 v58, v58, v74\n"
"v_add_f32 v59, v59, v75\n"
"v_mfma_f32_16x16x32_bf16 v[24:27], a[136:139], v[136:139], v[24:27]\n"
"s_waitcnt lgkmcnt(4)\n"
"v_add_f32 v60, v60, v76\n"
"v_add_f32 v61, v61, v77\n"
"v_mfma_f32_16x16x32_bf16 v[28:31], a[140:143], v[136:139], v[28:31]\n"
"s_waitcnt vmcnt(6)\n"
"v_add_f32 v62, v62, v78\n"
"v_add_f32 v63, v63, v79\n"
"v_mfma_f32_16x16x32_bf16 v[16:19], a[144:147], v[140:143], v[16:19]\n"
"s_waitcnt lgkmcnt(3)\n"
"v_add_f32 v48, v48, v80\n"
"v_add_f32 v49, v49, v81\n"
"v_mfma_f32_16x16x32_bf16 v[20:23], a[148:151], v[140:143], v[20:23]\n"
"v_add_f32 v50, v50, v82\n"
"v_add_f32 v51, v51, v83\n"
"v_mfma_f32_16x16x32_bf16 v[24:27], a[152:155], v[140:143], v[24:27]\n"
"s_waitcnt lgkmcnt(2)\n"
"v_add_f32 v52, v52, v84\n"
"v_add_f32 v53, v53, v85\n"
"v_mfma_f32_16x16x32_bf16 v[28:31], a[156:159], v[140:143], v[28:31]\n"
"s_waitcnt vmcnt(3)\n"
"v_add_f32 v54, v54, v86\n"
"v_add_f32 v55, v55, v87\n"
"v_mfma_f32_16x16x32_bf16 v[16:19], a[160:163], v[144:147], v[16:19]\n"
"s_waitcnt lgkmcnt(1)\n"
"v_add_f32 v56, v56, v88\n"
"v_add_f32 v57, v57, v89\n"
"v_mfma_f32_16x16x32_bf16 v[20:23], a[164:167], v[144:147], v[20:23]\n"
"v_add_f32 v58, v58, v90\n"
"v_add_f32 v59, v59, v91\n"
"v_mfma_f32_16x16x32_bf16 v[24:27], a[168:171], v[144:147], v[24:27]\n"
"s_waitcnt lgkmcnt(0)\n"
"v_add_f32 v60, v60, v92\n"
"v_add_f32 v61, v61, v93\n"
"v_mfma_f32_16x16x32_bf16 v[28:31], a[172:175], v[144:147], v[28:31]\n"
"s_waitcnt vmcnt(2)\n"
"v_add_f32 v62, v62, v94\n"
"v_add_f32 v63, v63, v95\n"
"v_mfma_f32_16x16x32_bf16 v[16:19], a[192:195], v[148:151], v[16:19]\n"
"v_mfma_f32_16x16x32_bf16 v[20:23], a[196:199], v[148:151], v[20:23]\n"
"v_mfma_f32_16x16x32_bf16 v[24:27], a[200:203], v[148:151], v[24:27]\n"
"v_mfma_f32_16x16x32_bf16 v[28:31], a[204:207], v[148:151], v[28:31]\n"
"s_waitcnt vmcnt(1)\n"
"v_mfma_f32_16x16x32_bf16 v[16:19], a[208:211], v[152:155], v[16:19]\n"
"v_lshlrev_b32 v96, 16, v112\n"
"v_and_b32 v97, 0xffff0000, v112\n"
"v_mfma_f32_16x16x32_bf16 v[20:23], a[212:215], v[152:155], v[20:23]\n"
"v_lshlrev_b32 v98, 16, v113\n"
"v_and_b32 v99, 0xffff0000, v113\n"
"v_mfma_f32_16x16x32_bf16 v[24:27], a[216:219], v[152:155], v[24:27]\n"
"v_lshlrev_b32 v100, 16, v114\n"
"v_and_b32 v101, 0xffff0000, v114\n"
"v_mfma_f32_16x16x32_bf16 v[28:31], a[220:223], v[152:155], v[28:31]\n"
"s_waitcnt vmcnt(0)\n"
"v_lshlrev_b32 v102, 16, v115\n"
"v_and_b32 v103, 0xffff0000, v115\n"
"v_mfma_f32_16x16x32_bf16 v[16:19], a[224:227], v[156:159], v[16:19]\n"
"v_lshlrev_b32 v104, 16, v116\n"
"v_and_b32 v105, 0xffff0000, v116\n"
"v_mfma_f32_16x16x32_bf16 v[20:23], a[228:231], v[156:159], v[20:23]\n"
"v_lshlrev_b32 v106, 16, v117\n"
"v_and_b32 v107, 0xffff0000, v117\n"
"v_mfma_f32_16x16x32_bf16 v[24:27], a[232:235], v[156:159], v[24:27]\n"
"v_lshlrev_b32 v108, 16, v118\n"
"v_and_b32 v109, 0xffff0000, v118\n"
"v_mfma_f32_16x16x32_bf16 v[28:31], a[236:239], v[156:159], v[28:31]\n"
"v_lshlrev_b32 v110, 16, v119\n"
"v_and_b32 v111, 0xffff0000, v119\n"
"v_add_f32 v48, v48, v16\n"
"v_add_f32 v49, v49, v17\n"
"v_add_f32 v50, v50, v18\n"
"v_add_f32 v51, v51, v19\n"
"v_add_f32 v52, v52, v20\n"
"v_add_f32 v53, v53, v21\n"
"v_add_f32 v54, v54, v22\n"
"v_add_f32 v55, v55, v23\n"
"v_add_f32 v56, v56, v24\n"
"v_add_f32 v57, v57, v25\n"
"v_add_f32 v58, v58, v26\n"
"v_add_f32 v59, v59, v27\n"
"v_add_f32 v60, v60, v28\n"
"v_add_f32 v61, v61, v29\n"
"v_add_f32 v62, v62, v30\n"
"v_add_f32 v63, v63, v31\n"
"v_add_f32 v48, v48, v96\n"
"v_add_f32 v49, v49, v97\n"
"v_add_f32 v50, v50, v98\n"
"v_add_f32 v51, v51, v99\n"
"v_add_f32 v52, v52, v100\n"
"v_add_f32 v53, v53, v101\n"
"v_add_f32 v54, v54, v102\n"
"v_add_f32 v55, v55, v103\n"
"v_add_f32 v56, v56, v104\n"
"v_add_f32 v57, v57, v105\n"
"v_add_f32 v58, v58, v106\n"
"v_add_f32 v59, v59, v107\n"
"v_add_f32 v60, v60, v108\n"
"v_add_f32 v61, v61, v109\n"
"v_add_f32 v62, v62, v110\n"
"v_add_f32 v63, v63, v111\n"
"v_cvt_pk_bf16_f32 v48, v48, v49\n"
"v_cvt_pk_bf16_f32 v49, v50, v51\n"
"v_cvt_pk_bf16_f32 v50, v52, v53\n"
"v_cvt_pk_bf16_f32 v51, v54, v55\n"
"v_cvt_pk_bf16_f32 v56, v56, v57\n"
"v_cvt_pk_bf16_f32 v57, v58, v59\n"
"v_cvt_pk_bf16_f32 v58, v60, v61\n"
"v_cvt_pk_bf16_f32 v59, v62, v63\n"
"v_permlane32_swap_b32 v48, v50\n"
"v_permlane32_swap_b32 v49, v51\n"
"v_permlane32_swap_b32 v56, v58\n"
"v_permlane32_swap_b32 v57, v59\n"
"v_permlane16_swap_b32 v48, v50\n"
"v_permlane16_swap_b32 v49, v51\n"
"v_permlane16_swap_b32 v56, v58\n"
"v_permlane16_swap_b32 v57, v59\n"
"buffer_store_dwordx4 v[48:51], v240, s[36:39], 0 offen sc0 sc1\n"
"buffer_store_dwordx4 v[56:59], v240, s[36:39], 0 offen offset:64 sc0 sc1\n"
"s_endpgm\n"
"L_xslow_%=:\n"
"s_mov_b32 s73, 0x100000\n"
"L_xloop_%=:\n"
"buffer_load_dword v241, v244, s[60:63], s22 offen sc1\n"
"s_waitcnt vmcnt(0)\n"
"v_cmp_ne_u32 vcc, 1, v241\n"
"s_sub_u32 s73, s73, 1\n"
"s_cbranch_scc1 L_xback_%=\n"
"s_nop 1\n"
"s_cbranch_vccnz L_xloop_%=\n"
"s_branch L_xback_%=\n"
"L_o2slow_%=:\n"
"s_mov_b32 s73, 0x100000\n"
"L_o2loop_%=:\n"
"buffer_load_dword v241, v237, s[60:63], s21 offen sc1\n"
"s_waitcnt vmcnt(0)\n"
"v_cmp_ne_u32 vcc, 2, v241\n"
"s_sub_u32 s73, s73, 1\n"
"s_cbranch_scc1 L_go2_%=\n"
"s_nop 1\n"
"s_cbranch_vccnz L_o2loop_%=\n"
"s_branch L_go2_%=\n"
"L_oslow_%=:\n"
"s_mov_b32 s73, 0x100000\n"
"L_oloop_%=:\n"
"buffer_load_dword v241, v237, s[60:63], s21 offen sc1\n"
"s_waitcnt vmcnt(0)\n"
"v_cmp_eq_u32 vcc, 0, v241\n"
"s_sub_u32 s73, s73, 1\n"
"s_cbranch_scc1 L_go_%=\n"
"s_nop 1\n"
"s_cbranch_vccnz L_oloop_%=\n"
"s_branch L_go_%=\n"
"L_fin_%=:\n"
"L_hslow_%=:\n"
"s_mov_b32 s73, 0x100000\n"
"L_hloop_%=:\n"
"s_sleep 2\n"
"buffer_load_dword v255, v254, s[60:63], s75 offen sc1\n"
"s_waitcnt vmcnt(0)\n"
"v_cmp_ne_u32 vcc, 1, v255\n"
"s_sub_u32 s73, s73, 1\n"
"s_cbranch_scc1 L_hback_%=\n"
"s_nop 1\n"
"s_cbranch_vccnz L_hloop_%=\n"
"s_branch L_hback_%=\n"
"L_slow_%=:\n"
"s_waitcnt vmcnt(0) lgkmcnt(0)\n"
"s_nop 7\n"
"s_lshl_b32 s68, s29, 5\n"
"s_and_b32 s70, s29, 1\n"
"s_lshl_b32 s70, s70, 6\n"
"v_subrev_u32 v176, s20, v246\n"
"v_xor_b32 v176, s68, v176\n"
"v_subrev_u32 v180, s20, v247\n"
"v_xor_b32 v180, s70, v180\n"
"v_xor_b32 v181, 64, v180\n"
"v_xor_b32 v177, 32, v176\n"
"v_xor_b32 v178, 64, v176\n"
"v_xor_b32 v179, 96, v176\n"
"s_add_u32 s68, s33, 98304\n"
"v_and_b32 v182, 0xfff, v240\n"
"v_and_b32 v183, 0xfff, v241\n"
"v_add_u32 v182, s68, v182\n"
"v_add_u32 v183, s68, v183\n"
"v_and_b32 v184, 0xfff, v242\n"
"v_add_u32 v184, s68, v184\n"
"v_and_b32 v185, 0xfff, v243\n"
"v_add_u32 v185, s68, v185\n"
"v_and_b32 v186, 0xfff, v244\n"
"v_add_u32 v186, s68, v186\n"
"v_and_b32 v187, 0xfff, v245\n"
"v_add_u32 v187, s68, v187\n"
"v_mov_b32 v160, 0xff800000\n"
"v_mov_b32 v164, 0xff800000\n"
"v_mov_b32 v168, 0xff800000\n"
"v_mov_b32 v172, 0xff800000\n"
"s_mov_b32 s25, 0\n"
"L_s1_%=:\n"
"s_add_u32 m0, s33, 98304\n"
"s_add_u32 s72, s25, 0\n"
"buffer_load_dwordx4 v176, s[44:47], s72 offen lds\n"
"s_add_u32 m0, s33, 99328\n"
"s_add_u32 s72, s25, 1024\n"
"buffer_load_dwordx4 v177, s[44:47], s72 offen lds\n"
"s_add_u32 m0, s33, 100352\n"
"s_add_u32 s72, s25, 2048\n"
"buffer_load_dwordx4 v178, s[44:47], s72 offen lds\n"
"s_add_u32 m0, s33, 101376\n"
"s_add_u32 s72, s25, 3072\n"
"buffer_load_dwordx4 v179, s[44:47], s72 offen lds\n"
"s_add_u32 m0, s33, 114688\n"
"s_add_u32 s72, s25, 4096\n"
"buffer_load_dwordx4 v176, s[44:47], s72 offen lds\n"
"s_add_u32 m0, s33, 115712\n"
"s_add_u32 s72, s25, 5120\n"
"buffer_load_dwordx4 v177, s[44:47], s72 offen lds\n"
"s_add_u32 m0, s33, 116736\n"
"s_add_u32 s72, s25, 6144\n"
"buffer_load_dwordx4 v178, s[44:47], s72 offen lds\n"
"s_add_u32 m0, s33, 117760\n"
"s_add_u32 s72, s25, 7168\n"
"buffer_load_dwordx4 v179, s[44:47], s72 offen lds\n"
"s_waitcnt vmcnt(0)\n"
"ds_read_b128 a[192:195], v182 offset:0\n"
"ds_read_b128 a[196:199], v183 offset:0\n"
"ds_read_b128 a[200:203], v182 offset:512\n"
"ds_read_b128 a[204:207], v183 offset:512\n"
"ds_read_b128 a[208:211], v182 offset:16384\n"
"ds_read_b128 a[212:215], v183 offset:16384\n"
"ds_read_b128 a[216:219], v182 offset:16896\n"
"ds_read_b128 a[220:223], v183 offset:16896\n"
"s_waitcnt lgkmcnt(0)\n"
"v_mfma_f32_16x16x32_bf16 v[64:67], a[192:195], v[0:3], 0\n"
"v_mfma_f32_16x16x32_bf16 v[68:71], a[192:195], v[4:7], 0\n"
"v_mfma_f32_16x16x32_bf16 v[72:75], a[192:195], v[8:11], 0\n"
"v_mfma_f32_16x16x32_bf16 v[76:79], a[192:195], v[12:15], 0\n"
"v_mfma_f32_16x16x32_bf16 v[64:67], a[196:199], v[16:19], v[64:67]\n"
"v_mfma_f32_16x16x32_bf16 v[68:71], a[196:199], v[20:23], v[68:71]\n"
"v_mfma_f32_16x16x32_bf16 v[72:75], a[196:199], v[24:27], v[72:75]\n"
"v_mfma_f32_16x16x32_bf16 v[76:79], a[196:199], v[28:31], v[76:79]\n"
"v_mfma_f32_16x16x32_bf16 v[64:67], a[200:203], v[32:35], v[64:67]\n"
"v_mfma_f32_16x16x32_bf16 v[68:71], a[200:203], v[36:39], v[68:71]\n"
"v_mfma_f32_16x16x32_bf16 v[72:75], a[200:203], v[40:43], v[72:75]\n"
"v_mfma_f32_16x16x32_bf16 v[76:79], a[200:203], v[44:47], v[76:79]\n"
"v_mfma_f32_16x16x32_bf16 v[64:67], a[204:207], v[48:51], v[64:67]\n"
"v_mfma_f32_16x16x32_bf16 v[68:71], a[204:207], v[52:55], v[68:71]\n"
"v_mfma_f32_16x16x32_bf16 v[72:75], a[204:207], v[56:59], v[72:75]\n"
"v_mfma_f32_16x16x32_bf16 v[76:79], a[204:207], v[60:63], v[76:79]\n"
"v_mfma_f32_16x16x32_bf16 v[80:83], a[208:211], v[0:3], 0\n"
"v_mfma_f32_16x16x32_bf16 v[84:87], a[208:211], v[4:7], 0\n"
"v_mfma_f32_16x16x32_bf16 v[88:91], a[208:211], v[8:11], 0\n"
"v_mfma_f32_16x16x32_bf16 v[92:95], a[208:211], v[12:15], 0\n"
"v_mfma_f32_16x16x32_bf16 v[80:83], a[212:215], v[16:19], v[80:83]\n"
"v_mfma_f32_16x16x32_bf16 v[84:87], a[212:215], v[20:23], v[84:87]\n"
"v_mfma_f32_16x16x32_bf16 v[88:91], a[212:215], v[24:27], v[88:91]\n"
"v_mfma_f32_16x16x32_bf16 v[92:95], a[212:215], v[28:31], v[92:95]\n"
"v_mfma_f32_16x16x32_bf16 v[80:83], a[216:219], v[32:35], v[80:83]\n"
"v_mfma_f32_16x16x32_bf16 v[84:87], a[216:219], v[36:39], v[84:87]\n"
"v_mfma_f32_16x16x32_bf16 v[88:91], a[216:219], v[40:43], v[88:91]\n"
"v_mfma_f32_16x16x32_bf16 v[92:95], a[216:219], v[44:47], v[92:95]\n"
"v_mfma_f32_16x16x32_bf16 v[80:83], a[220:223], v[48:51], v[80:83]\n"
"v_mfma_f32_16x16x32_bf16 v[84:87], a[220:223], v[52:55], v[84:87]\n"
"v_mfma_f32_16x16x32_bf16 v[88:91], a[220:223], v[56:59], v[88:91]\n"
"v_mfma_f32_16x16x32_bf16 v[92:95], a[220:223], v[60:63], v[92:95]\n"
"s_nop 7\n"
"v_max3_f32 v160, v160, v64, v65\n"
"v_max3_f32 v160, v160, v66, v67\n"
"v_max3_f32 v160, v160, v80, v81\n"
"v_max3_f32 v160, v160, v82, v83\n"
"v_max3_f32 v164, v164, v68, v69\n"
"v_max3_f32 v164, v164, v70, v71\n"
"v_max3_f32 v164, v164, v84, v85\n"
"v_max3_f32 v164, v164, v86, v87\n"
"v_max3_f32 v168, v168, v72, v73\n"
"v_max3_f32 v168, v168, v74, v75\n"
"v_max3_f32 v168, v168, v88, v89\n"
"v_max3_f32 v168, v168, v90, v91\n"
"v_max3_f32 v172, v172, v76, v77\n"
"v_max3_f32 v172, v172, v78, v79\n"
"v_max3_f32 v172, v172, v92, v93\n"
"v_max3_f32 v172, v172, v94, v95\n"
"s_add_u32 s25, s25, 8192\n"
"s_cmp_lt_u32 s25, 0x40000\n"
"s_cbranch_scc1 L_s1_%=\n"
"v_mov_b32 v161, v160\n"
"v_mov_b32 v165, v164\n"
"v_mov_b32 v169, v168\n"
"v_mov_b32 v173, v172\n"
"v_permlane32_swap_b32 v160, v161\n"
"v_permlane32_swap_b32 v164, v165\n"
"v_permlane32_swap_b32 v168, v169\n"
"v_permlane32_swap_b32 v172, v173\n"
"v_max_f32 v160, v160, v161\n"
"v_mov_b32 v161, v160\n"
"v_max_f32 v164, v164, v165\n"
"v_mov_b32 v165, v164\n"
"v_max_f32 v168, v168, v169\n"
"v_mov_b32 v169, v168\n"
"v_max_f32 v172, v172, v173\n"
"v_mov_b32 v173, v172\n"
"v_permlane16_swap_b32 v160, v161\n"
"v_permlane16_swap_b32 v164, v165\n"
"v_permlane16_swap_b32 v168, v169\n"
"v_permlane16_swap_b32 v172, v173\n"
"v_min_f32 v160, -v160, -v161\n"
"v_min_f32 v164, -v164, -v165\n"
"v_min_f32 v168, -v168, -v169\n"
"v_min_f32 v172, -v172, -v173\n"
"v_mov_b32 v161, v160\n"
"v_mov_b32 v162, v160\n"
"v_mov_b32 v163, v160\n"
"v_mov_b32 v165, v164\n"
"v_mov_b32 v166, v164\n"
"v_mov_b32 v167, v164\n"
"v_mov_b32 v169, v168\n"
"v_mov_b32 v170, v168\n"
"v_mov_b32 v171, v168\n"
"v_mov_b32 v173, v172\n"
"v_mov_b32 v174, v172\n"
"v_mov_b32 v175, v172\n"
"v_accvgpr_write_b32 a0, 0\n"
"v_accvgpr_write_b32 a1, 0\n"
"v_accvgpr_write_b32 a2, 0\n"
"v_accvgpr_write_b32 a3, 0\n"
"v_accvgpr_write_b32 a4, 0\n"
"v_accvgpr_write_b32 a5, 0\n"
"v_accvgpr_write_b32 a6, 0\n"
"v_accvgpr_write_b32 a7, 0\n"
"v_accvgpr_write_b32 a8, 0\n"
"v_accvgpr_write_b32 a9, 0\n"
"v_accvgpr_write_b32 a10, 0\n"
"v_accvgpr_write_b32 a11, 0\n"
"v_accvgpr_write_b32 a12, 0\n"
"v_accvgpr_write_b32 a13, 0\n"
"v_accvgpr_write_b32 a14, 0\n"
"v_accvgpr_write_b32 a15, 0\n"
"v_accvgpr_write_b32 a16, 0\n"
"v_accvgpr_write_b32 a17, 0\n"
"v_accvgpr_write_b32 a18, 0\n"
"v_accvgpr_write_b32 a19, 0\n"
"v_accvgpr_write_b32 a20, 0\n"
"v_accvgpr_write_b32 a21, 0\n"
"v_accvgpr_write_b32 a22, 0\n"
"v_accvgpr_write_b32 a23, 0\n"
"v_accvgpr_write_b32 a24, 0\n"
"v_accvgpr_write_b32 a25, 0\n"
"v_accvgpr_write_b32 a26, 0\n"
"v_accvgpr_write_b32 a27, 0\n"
"v_accvgpr_write_b32 a28, 0\n"
"v_accvgpr_write_b32 a29, 0\n"
"v_accvgpr_write_b32 a30, 0\n"
"v_accvgpr_write_b32 a31, 0\n"
"v_accvgpr_write_b32 a32, 0\n"
"v_accvgpr_write_b32 a33, 0\n"
"v_accvgpr_write_b32 a34, 0\n"
"v_accvgpr_write_b32 a35, 0\n"
"v_accvgpr_write_b32 a36, 0\n"
"v_accvgpr_write_b32 a37, 0\n"
"v_accvgpr_write_b32 a38, 0\n"
"v_accvgpr_write_b32 a39, 0\n"
"v_accvgpr_write_b32 a40, 0\n"
"v_accvgpr_write_b32 a41, 0\n"
"v_accvgpr_write_b32 a42, 0\n"
"v_accvgpr_write_b32 a43, 0\n"
"v_accvgpr_write_b32 a44, 0\n"
"v_accvgpr_write_b32 a45, 0\n"
"v_accvgpr_write_b32 a46, 0\n"
"v_accvgpr_write_b32 a47, 0\n"
"v_accvgpr_write_b32 a48, 0\n"
"v_accvgpr_write_b32 a49, 0\n"
"v_accvgpr_write_b32 a50, 0\n"
"v_accvgpr_write_b32 a51, 0\n"
"v_accvgpr_write_b32 a52, 0\n"
"v_accvgpr_write_b32 a53, 0\n"
"v_accvgpr_write_b32 a54, 0\n"
"v_accvgpr_write_b32 a55, 0\n"
"v_accvgpr_write_b32 a56, 0\n"
"v_accvgpr_write_b32 a57, 0\n"
"v_accvgpr_write_b32 a58, 0\n"
"v_accvgpr_write_b32 a59, 0\n"
"v_accvgpr_write_b32 a60, 0\n"
"v_accvgpr_write_b32 a61, 0\n"
"v_accvgpr_write_b32 a62, 0\n"
"v_accvgpr_write_b32 a63, 0\n"
"v_accvgpr_write_b32 a64, 0\n"
"v_accvgpr_write_b32 a65, 0\n"
"v_accvgpr_write_b32 a66, 0\n"
"v_accvgpr_write_b32 a67, 0\n"
"v_accvgpr_write_b32 a68, 0\n"
"v_accvgpr_write_b32 a69, 0\n"
"v_accvgpr_write_b32 a70, 0\n"
"v_accvgpr_write_b32 a71, 0\n"
"v_accvgpr_write_b32 a72, 0\n"
"v_accvgpr_write_b32 a73, 0\n"
"v_accvgpr_write_b32 a74, 0\n"
"v_accvgpr_write_b32 a75, 0\n"
"v_accvgpr_write_b32 a76, 0\n"
"v_accvgpr_write_b32 a77, 0\n"
"v_accvgpr_write_b32 a78, 0\n"
"v_accvgpr_write_b32 a79, 0\n"
"v_accvgpr_write_b32 a80, 0\n"
"v_accvgpr_write_b32 a81, 0\n"
"v_accvgpr_write_b32 a82, 0\n"
"v_accvgpr_write_b32 a83, 0\n"
"v_accvgpr_write_b32 a84, 0\n"
"v_accvgpr_write_b32 a85, 0\n"
"v_accvgpr_write_b32 a86, 0\n"
"v_accvgpr_write_b32 a87, 0\n"
"v_accvgpr_write_b32 a88, 0\n"
"v_accvgpr_write_b32 a89, 0\n"
"v_accvgpr_write_b32 a90, 0\n"
"v_accvgpr_write_b32 a91, 0\n"
"v_accvgpr_write_b32 a92, 0\n"
"v_accvgpr_write_b32 a93, 0\n"
"v_accvgpr_write_b32 a94, 0\n"
"v_accvgpr_write_b32 a95, 0\n"
"v_accvgpr_write_b32 a96, 0\n"
"v_accvgpr_write_b32 a97, 0\n"
"v_accvgpr_write_b32 a98, 0\n"
"v_accvgpr_write_b32 a99, 0\n"
"v_accvgpr_write_b32 a100, 0\n"
"v_accvgpr_write_b32 a101, 0\n"
"v_accvgpr_write_b32 a102, 0\n"
"v_accvgpr_write_b32 a103, 0\n"
"v_accvgpr_write_b32 a104, 0\n"
"v_accvgpr_write_b32 a105, 0\n"
"v_accvgpr_write_b32 a106, 0\n"
"v_accvgpr_write_b32 a107, 0\n"
"v_accvgpr_write_b32 a108, 0\n"
"v_accvgpr_write_b32 a109, 0\n"
"v_accvgpr_write_b32 a110, 0\n"
"v_accvgpr_write_b32 a111, 0\n"
"v_accvgpr_write_b32 a112, 0\n"
"v_accvgpr_write_b32 a113, 0\n"
"v_accvgpr_write_b32 a114, 0\n"
"v_accvgpr_write_b32 a115, 0\n"
"v_accvgpr_write_b32 a116, 0\n"
"v_accvgpr_write_b32 a117, 0\n"
"v_accvgpr_write_b32 a118, 0\n"
"v_accvgpr_write_b32 a119, 0\n"
"v_accvgpr_write_b32 a120, 0\n"
"v_accvgpr_write_b32 a121, 0\n"
"v_accvgpr_write_b32 a122, 0\n"
"v_accvgpr_write_b32 a123, 0\n"
"v_accvgpr_write_b32 a124, 0\n"
"v_accvgpr_write_b32 a125, 0\n"
"v_accvgpr_write_b32 a126, 0\n"
"v_accvgpr_write_b32 a127, 0\n"
"v_accvgpr_write_b32 a128, 0\n"
"v_accvgpr_write_b32 a129, 0\n"
"v_accvgpr_write_b32 a130, 0\n"
"v_accvgpr_write_b32 a131, 0\n"
"v_accvgpr_write_b32 a132, 0\n"
"v_accvgpr_write_b32 a133, 0\n"
"v_accvgpr_write_b32 a134, 0\n"
"v_accvgpr_write_b32 a135, 0\n"
"v_accvgpr_write_b32 a136, 0\n"
"v_accvgpr_write_b32 a137, 0\n"
"v_accvgpr_write_b32 a138, 0\n"
"v_accvgpr_write_b32 a139, 0\n"
"v_accvgpr_write_b32 a140, 0\n"
"v_accvgpr_write_b32 a141, 0\n"
"v_accvgpr_write_b32 a142, 0\n"
"v_accvgpr_write_b32 a143, 0\n"
"s_mov_b32 s25, 0\n"
"L_s2_%=:\n"
"s_add_u32 m0, s33, 98304\n"
"s_add_u32 s72, s25, 0\n"
"buffer_load_dwordx4 v176, s[44:47], s72 offen lds\n"
"s_add_u32 m0, s33, 99328\n"
"s_add_u32 s72, s25, 1024\n"
"buffer_load_dwordx4 v177, s[44:47], s72 offen lds\n"
"s_add_u32 m0, s33, 100352\n"
"s_add_u32 s72, s25, 2048\n"
"buffer_load_dwordx4 v178, s[44:47], s72 offen lds\n"
"s_add_u32 m0, s33, 101376\n"
"s_add_u32 s72, s25, 3072\n"
"buffer_load_dwordx4 v179, s[44:47], s72 offen lds\n"
"s_add_u32 m0, s33, 114688\n"
"s_add_u32 s72, s25, 4096\n"
"buffer_load_dwordx4 v176, s[44:47], s72 offen lds\n"
"s_add_u32 m0, s33, 115712\n"
"s_add_u32 s72, s25, 5120\n"
"buffer_load_dwordx4 v177, s[44:47], s72 offen lds\n"
"s_add_u32 m0, s33, 116736\n"
"s_add_u32 s72, s25, 6144\n"
"buffer_load_dwordx4 v178, s[44:47], s72 offen lds\n"
"s_add_u32 m0, s33, 117760\n"
"s_add_u32 s72, s25, 7168\n"
"buffer_load_dwordx4 v179, s[44:47], s72 offen lds\n"
"s_waitcnt vmcnt(0)\n"
"ds_read_b128 a[192:195], v182 offset:0\n"
"ds_read_b128 a[196:199], v183 offset:0\n"
"ds_read_b128 a[200:203], v182 offset:512\n"
"ds_read_b128 a[204:207], v183 offset:512\n"
"ds_read_b128 a[208:211], v182 offset:16384\n"
"ds_read_b128 a[212:215], v183 offset:16384\n"
"ds_read_b128 a[216:219], v182 offset:16896\n"
"ds_read_b128 a[220:223], v183 offset:16896\n"
"s_waitcnt lgkmcnt(0)\n"
"v_mfma_f32_16x16x32_bf16 v[64:67], a[192:195], v[0:3], v[160:163]\n"
"v_mfma_f32_16x16x32_bf16 v[68:71], a[192:195], v[4:7], v[164:167]\n"
"v_mfma_f32_16x16x32_bf16 v[72:75], a[192:195], v[8:11], v[168:171]\n"
"v_mfma_f32_16x16x32_bf16 v[76:79], a[192:195], v[12:15], v[172:175]\n"
"v_mfma_f32_16x16x32_bf16 v[64:67], a[196:199], v[16:19], v[64:67]\n"
"v_mfma_f32_16x16x32_bf16 v[68:71], a[196:199], v[20:23], v[68:71]\n"
"v_mfma_f32_16x16x32_bf16 v[72:75], a[196:199], v[24:27], v[72:75]\n"
"v_mfma_f32_16x16x32_bf16 v[76:79], a[196:199], v[28:31], v[76:79]\n"
"v_mfma_f32_16x16x32_bf16 v[64:67], a[200:203], v[32:35], v[64:67]\n"
"v_mfma_f32_16x16x32_bf16 v[68:71], a[200:203], v[36:39], v[68:71]\n"
"v_mfma_f32_16x16x32_bf16 v[72:75], a[200:203], v[40:43], v[72:75]\n"
"v_mfma_f32_16x16x32_bf16 v[76:79], a[200:203], v[44:47], v[76:79]\n"
"v_mfma_f32_16x16x32_bf16 v[64:67], a[204:207], v[48:51], v[64:67]\n"
"v_mfma_f32_16x16x32_bf16 v[68:71], a[204:207], v[52:55], v[68:71]\n"
"v_mfma_f32_16x16x32_bf16 v[72:75], a[204:207], v[56:59], v[72:75]\n"
"v_mfma_f32_16x16x32_bf16 v[76:79], a[204:207], v[60:63], v[76:79]\n"
"v_mfma_f32_16x16x32_bf16 v[80:83], a[208:211], v[0:3], v[160:163]\n"
"v_mfma_f32_16x16x32_bf16 v[84:87], a[208:211], v[4:7], v[164:167]\n"
"v_mfma_f32_16x16x32_bf16 v[88:91], a[208:211], v[8:11], v[168:171]\n"
"v_mfma_f32_16x16x32_bf16 v[92:95], a[208:211], v[12:15], v[172:175]\n"
"v_mfma_f32_16x16x32_bf16 v[80:83], a[212:215], v[16:19], v[80:83]\n"
"v_mfma_f32_16x16x32_bf16 v[84:87], a[212:215], v[20:23], v[84:87]\n"
"v_mfma_f32_16x16x32_bf16 v[88:91], a[212:215], v[24:27], v[88:91]\n"
"v_mfma_f32_16x16x32_bf16 v[92:95], a[212:215], v[28:31], v[92:95]\n"
"v_mfma_f32_16x16x32_bf16 v[80:83], a[216:219], v[32:35], v[80:83]\n"
"v_mfma_f32_16x16x32_bf16 v[84:87], a[216:219], v[36:39], v[84:87]\n"
"v_mfma_f32_16x16x32_bf16 v[88:91], a[216:219], v[40:43], v[88:91]\n"
"v_mfma_f32_16x16x32_bf16 v[92:95], a[216:219], v[44:47], v[92:95]\n"
"v_mfma_f32_16x16x32_bf16 v[80:83], a[220:223], v[48:51], v[80:83]\n"
"v_mfma_f32_16x16x32_bf16 v[84:87], a[220:223], v[52:55], v[84:87]\n"
"v_mfma_f32_16x16x32_bf16 v[88:91], a[220:223], v[56:59], v[88:91]\n"
"v_mfma_f32_16x16x32_bf16 v[92:95], a[220:223], v[60:63], v[92:95]\n"
"s_nop 7\n"
"s_add_u32 m0, s33, 98304\n"
"s_add_u32 s72, s25, 0\n"
"buffer_load_dwordx4 v180, s[48:51], s72 offen lds\n"
"s_add_u32 m0, s33, 99328\n"
"s_add_u32 s72, s25, 1024\n"
"buffer_load_dwordx4 v181, s[48:51], s72 offen lds\n"
"s_add_u32 m0, s33, 100352\n"
"s_add_u32 s72, s25, 2048\n"
"buffer_load_dwordx4 v180, s[48:51], s72 offen lds\n"
"s_add_u32 m0, s33, 101376\n"
"s_add_u32 s72, s25, 3072\n"
"buffer_load_dwordx4 v181, s[48:51], s72 offen lds\n"
"s_add_u32 m0, s33, 114688\n"
"s_add_u32 s72, s25, 4096\n"
"buffer_load_dwordx4 v180, s[48:51], s72 offen lds\n"
"s_add_u32 m0, s33, 115712\n"
"s_add_u32 s72, s25, 5120\n"
"buffer_load_dwordx4 v181, s[48:51], s72 offen lds\n"
"s_add_u32 m0, s33, 116736\n"
"s_add_u32 s72, s25, 6144\n"
"buffer_load_dwordx4 v180, s[48:51], s72 offen lds\n"
"s_add_u32 m0, s33, 117760\n"
"s_add_u32 s72, s25, 7168\n"
"buffer_load_dwordx4 v181, s[48:51], s72 offen lds\n"
"s_waitcnt vmcnt(0)\n"
"v_exp_f32 v64, v64\n"
"v_exp_f32 v65, v65\n"
"v_exp_f32 v66, v66\n"
"v_exp_f32 v67, v67\n"
"v_exp_f32 v68, v68\n"
"v_exp_f32 v69, v69\n"
"v_exp_f32 v70, v70\n"
"v_exp_f32 v71, v71\n"
"v_exp_f32 v72, v72\n"
"v_exp_f32 v73, v73\n"
"v_exp_f32 v74, v74\n"
"v_exp_f32 v75, v75\n"
"v_exp_f32 v76, v76\n"
"v_exp_f32 v77, v77\n"
"v_exp_f32 v78, v78\n"
"v_exp_f32 v79, v79\n"
"v_cvt_pk_bf16_f32 v128, v64, v65\n"
"v_cvt_pk_bf16_f32 v129, v66, v67\n"
"v_cvt_pk_bf16_f32 v132, v68, v69\n"
"v_cvt_pk_bf16_f32 v133, v70, v71\n"
"v_cvt_pk_bf16_f32 v136, v72, v73\n"
"v_cvt_pk_bf16_f32 v137, v74, v75\n"
"v_cvt_pk_bf16_f32 v140, v76, v77\n"
"v_cvt_pk_bf16_f32 v141, v78, v79\n"
"v_exp_f32 v80, v80\n"
"v_exp_f32 v81, v81\n"
"v_exp_f32 v82, v82\n"
"v_exp_f32 v83, v83\n"
"v_exp_f32 v84, v84\n"
"v_exp_f32 v85, v85\n"
"v_exp_f32 v86, v86\n"
"v_exp_f32 v87, v87\n"
"v_exp_f32 v88, v88\n"
"v_exp_f32 v89, v89\n"
"v_exp_f32 v90, v90\n"
"v_exp_f32 v91, v91\n"
"v_exp_f32 v92, v92\n"
"v_exp_f32 v93, v93\n"
"v_exp_f32 v94, v94\n"
"v_exp_f32 v95, v95\n"
"v_cvt_pk_bf16_f32 v130, v80, v81\n"
"v_cvt_pk_bf16_f32 v131, v82, v83\n"
"v_cvt_pk_bf16_f32 v134, v84, v85\n"
"v_cvt_pk_bf16_f32 v135, v86, v87\n"
"v_cvt_pk_bf16_f32 v138, v88, v89\n"
"v_cvt_pk_bf16_f32 v139, v90, v91\n"
"v_cvt_pk_bf16_f32 v142, v92, v93\n"
"v_cvt_pk_bf16_f32 v143, v94, v95\n"
"ds_read_b64_tr_b16 a[192:193], v184 offset:0\n"
"ds_read_b64_tr_b16 a[194:195], v184 offset:16384\n"
"ds_read_b64_tr_b16 a[196:197], v185 offset:0\n"
"ds_read_b64_tr_b16 a[198:199], v185 offset:16384\n"
"ds_read_b64_tr_b16 a[200:201], v186 offset:0\n"
"ds_read_b64_tr_b16 a[202:203], v186 offset:16384\n"
"ds_read_b64_tr_b16 a[204:205], v187 offset:0\n"
"ds_read_b64_tr_b16 a[206:207], v187 offset:16384\n"
"ds_read_b64_tr_b16 a[208:209], v184 offset:512\n"
"ds_read_b64_tr_b16 a[210:211], v184 offset:16896\n"
"ds_read_b64_tr_b16 a[212:213], v185 offset:512\n"
"ds_read_b64_tr_b16 a[214:215], v185 offset:16896\n"
"ds_read_b64_tr_b16 a[216:217], v186 offset:512\n"
"ds_read_b64_tr_b16 a[218:219], v186 offset:16896\n"
"ds_read_b64_tr_b16 a[220:221], v187 offset:512\n"
"ds_read_b64_tr_b16 a[222:223], v187 offset:16896\n"
"s_waitcnt lgkmcnt(0)\n"
"v_smfmac_f32_16x16x64_bf16 a[128:131], a[144:147], v[128:135], v239\n"
"v_smfmac_f32_16x16x64_bf16 a[132:135], a[144:147], v[136:143], v239\n"
"v_mfma_f32_16x16x32_bf16 a[0:3], a[192:195], v[128:131], a[0:3]\n"
"v_mfma_f32_16x16x32_bf16 a[4:7], a[192:195], v[132:135], a[4:7]\n"
"v_mfma_f32_16x16x32_bf16 a[8:11], a[192:195], v[136:139], a[8:11]\n"
"v_mfma_f32_16x16x32_bf16 a[12:15], a[192:195], v[140:143], a[12:15]\n"
"v_mfma_f32_16x16x32_bf16 a[16:19], a[196:199], v[128:131], a[16:19]\n"
"v_mfma_f32_16x16x32_bf16 a[20:23], a[196:199], v[132:135], a[20:23]\n"
"v_mfma_f32_16x16x32_bf16 a[24:27], a[196:199], v[136:139], a[24:27]\n"
"v_mfma_f32_16x16x32_bf16 a[28:31], a[196:199], v[140:143], a[28:31]\n"
"v_mfma_f32_16x16x32_bf16 a[32:35], a[200:203], v[128:131], a[32:35]\n"
"v_mfma_f32_16x16x32_bf16 a[36:39], a[200:203], v[132:135], a[36:39]\n"
"v_mfma_f32_16x16x32_bf16 a[40:43], a[200:203], v[136:139], a[40:43]\n"
"v_mfma_f32_16x16x32_bf16 a[44:47], a[200:203], v[140:143], a[44:47]\n"
"v_mfma_f32_16x16x32_bf16 a[48:51], a[204:207], v[128:131], a[48:51]\n"
"v_mfma_f32_16x16x32_bf16 a[52:55], a[204:207], v[132:135], a[52:55]\n"
"v_mfma_f32_16x16x32_bf16 a[56:59], a[204:207], v[136:139], a[56:59]\n"
"v_mfma_f32_16x16x32_bf16 a[60:63], a[204:207], v[140:143], a[60:63]\n"
"v_mfma_f32_16x16x32_bf16 a[64:67], a[208:211], v[128:131], a[64:67]\n"
"v_mfma_f32_16x16x32_bf16 a[68:71], a[208:211], v[132:135], a[68:71]\n"
"v_mfma_f32_16x16x32_bf16 a[72:75], a[208:211], v[136:139], a[72:75]\n"
"v_mfma_f32_16x16x32_bf16 a[76:79], a[208:211], v[140:143], a[76:79]\n"
"v_mfma_f32_16x16x32_bf16 a[80:83], a[212:215], v[128:131], a[80:83]\n"
"v_mfma_f32_16x16x32_bf16 a[84:87], a[212:215], v[132:135], a[84:87]\n"
"v_mfma_f32_16x16x32_bf16 a[88:91], a[212:215], v[136:139], a[88:91]\n"
"v_mfma_f32_16x16x32_bf16 a[92:95], a[212:215], v[140:143], a[92:95]\n"
"v_mfma_f32_16x16x32_bf16 a[96:99], a[216:219], v[128:131], a[96:99]\n"
"v_mfma_f32_16x16x32_bf16 a[100:103], a[216:219], v[132:135], a[100:103]\n"
"v_mfma_f32_16x16x32_bf16 a[104:107], a[216:219], v[136:139], a[104:107]\n"
"v_mfma_f32_16x16x32_bf16 a[108:111], a[216:219], v[140:143], a[108:111]\n"
"v_mfma_f32_16x16x32_bf16 a[112:115], a[220:223], v[128:131], a[112:115]\n"
"v_mfma_f32_16x16x32_bf16 a[116:119], a[220:223], v[132:135], a[116:119]\n"
"v_mfma_f32_16x16x32_bf16 a[120:123], a[220:223], v[136:139], a[120:123]\n"
"v_mfma_f32_16x16x32_bf16 a[124:127], a[220:223], v[140:143], a[124:127]\n"
"s_add_u32 s25, s25, 8192\n"
"s_cmp_lt_u32 s25, 0x40000\n"
"s_cbranch_scc1 L_s2_%=\n"
"s_add_u32 m0, s33, 114688\n"
"s_add_u32 s72, s80, 32768\n"
"buffer_load_dwordx4 v248, s[56:59], s72 offen lds\n"
"s_add_u32 m0, s33, 115712\n"
"s_add_u32 s72, s80, 40960\n"
"buffer_load_dwordx4 v248, s[56:59], s72 offen lds\n"
"s_add_u32 m0, s33, 116736\n"
"s_add_u32 s72, s80, 49152\n"
"buffer_load_dwordx4 v248, s[56:59], s72 offen lds\n"
"s_add_u32 m0, s33, 117760\n"
"s_add_u32 s72, s80, 57344\n"
"buffer_load_dwordx4 v248, s[56:59], s72 offen lds\n"
"s_add_u32 m0, s33, 98304\n"
"s_add_u32 s72, s80, 163840\n"
"buffer_load_dwordx4 v248, s[56:59], s72 offen lds\n"
"s_add_u32 m0, s33, 99328\n"
"s_add_u32 s72, s80, 172032\n"
"buffer_load_dwordx4 v248, s[56:59], s72 offen lds\n"
"s_add_u32 m0, s33, 100352\n"
"s_add_u32 s72, s80, 180224\n"
"buffer_load_dwordx4 v248, s[56:59], s72 offen lds\n"
"s_add_u32 m0, s33, 101376\n"
"s_add_u32 s72, s80, 188416\n"
"buffer_load_dwordx4 v248, s[56:59], s72 offen lds\n"
"s_waitcnt vmcnt(0)\n"
"s_nop 7\n"
"v_accvgpr_read_b32 v64, a128\n"
"v_accvgpr_read_b32 v65, a130\n"
"v_accvgpr_read_b32 v66, a132\n"
"v_accvgpr_read_b32 v67, a134\n"
"v_accvgpr_read_b32 v70, a129\n"
"v_accvgpr_read_b32 v71, a131\n"
"v_accvgpr_read_b32 v72, a133\n"
"v_accvgpr_read_b32 v73, a135\n"
"v_add_f32 v64, v64, v70\n"
"v_add_f32 v65, v65, v71\n"
"v_add_f32 v66, v66, v72\n"
"v_add_f32 v67, v67, v73\n"
"v_rcp_f32 v64, v64\n"
"v_rcp_f32 v65, v65\n"
"v_rcp_f32 v66, v66\n"
"v_rcp_f32 v67, v67\n"
"s_xor_b32 s25, s26, 4096\n"
"v_accvgpr_read_b32 v128, a0\n"
"v_accvgpr_read_b32 v129, a1\n"
"v_accvgpr_read_b32 v130, a2\n"
"v_accvgpr_read_b32 v131, a3\n"
"v_accvgpr_read_b32 v132, a16\n"
"v_accvgpr_read_b32 v133, a17\n"
"v_accvgpr_read_b32 v134, a18\n"
"v_accvgpr_read_b32 v135, a19\n"
"v_mul_f32 v128, v64, v128\n"
"v_mul_f32 v129, v64, v129\n"
"v_mul_f32 v130, v64, v130\n"
"v_mul_f32 v131, v64, v131\n"
"v_mul_f32 v132, v64, v132\n"
"v_mul_f32 v133, v64, v133\n"
"v_mul_f32 v134, v64, v134\n"
"v_mul_f32 v135, v64, v135\n"
"v_cvt_pk_bf16_f32 v144, v128, v129\n"
"v_cvt_pk_bf16_f32 v145, v130, v131\n"
"v_cvt_pk_bf16_f32 v146, v132, v133\n"
"v_cvt_pk_bf16_f32 v147, v134, v135\n"
"buffer_store_dwordx4 v[144:147], v251, s[52:55], s25 offen sc1\n"
"v_accvgpr_read_b32 v136, a32\n"
"v_accvgpr_read_b32 v137, a33\n"
"v_accvgpr_read_b32 v138, a34\n"
"v_accvgpr_read_b32 v139, a35\n"
"v_accvgpr_read_b32 v140, a48\n"
"v_accvgpr_read_b32 v141, a49\n"
"v_accvgpr_read_b32 v142, a50\n"
"v_accvgpr_read_b32 v143, a51\n"
"v_mul_f32 v136, v64, v136\n"
"v_mul_f32 v137, v64, v137\n"
"v_mul_f32 v138, v64, v138\n"
"v_mul_f32 v139, v64, v139\n"
"v_mul_f32 v140, v64, v140\n"
"v_mul_f32 v141, v64, v141\n"
"v_mul_f32 v142, v64, v142\n"
"v_mul_f32 v143, v64, v143\n"
"v_cvt_pk_bf16_f32 v148, v136, v137\n"
"v_cvt_pk_bf16_f32 v149, v138, v139\n"
"v_cvt_pk_bf16_f32 v150, v140, v141\n"
"v_cvt_pk_bf16_f32 v151, v142, v143\n"
"buffer_store_dwordx4 v[148:151], v251, s[52:55], s25 offen offset:1024 sc1\n"
"v_accvgpr_read_b32 v128, a64\n"
"v_accvgpr_read_b32 v129, a65\n"
"v_accvgpr_read_b32 v130, a66\n"
"v_accvgpr_read_b32 v131, a67\n"
"v_accvgpr_read_b32 v132, a80\n"
"v_accvgpr_read_b32 v133, a81\n"
"v_accvgpr_read_b32 v134, a82\n"
"v_accvgpr_read_b32 v135, a83\n"
"v_mul_f32 v128, v64, v128\n"
"v_mul_f32 v129, v64, v129\n"
"v_mul_f32 v130, v64, v130\n"
"v_mul_f32 v131, v64, v131\n"
"v_mul_f32 v132, v64, v132\n"
"v_mul_f32 v133, v64, v133\n"
"v_mul_f32 v134, v64, v134\n"
"v_mul_f32 v135, v64, v135\n"
"v_cvt_pk_bf16_f32 v152, v128, v129\n"
"v_cvt_pk_bf16_f32 v153, v130, v131\n"
"v_cvt_pk_bf16_f32 v154, v132, v133\n"
"v_cvt_pk_bf16_f32 v155, v134, v135\n"
"buffer_store_dwordx4 v[152:155], v251, s[52:55], s25 offen offset:2048 sc1\n"
"v_accvgpr_read_b32 v136, a96\n"
"v_accvgpr_read_b32 v137, a97\n"
"v_accvgpr_read_b32 v138, a98\n"
"v_accvgpr_read_b32 v139, a99\n"
"v_accvgpr_read_b32 v140, a112\n"
"v_accvgpr_read_b32 v141, a113\n"
"v_accvgpr_read_b32 v142, a114\n"
"v_accvgpr_read_b32 v143, a115\n"
"v_mul_f32 v136, v64, v136\n"
"v_mul_f32 v137, v64, v137\n"
"v_mul_f32 v138, v64, v138\n"
"v_mul_f32 v139, v64, v139\n"
"v_mul_f32 v140, v64, v140\n"
"v_mul_f32 v141, v64, v141\n"
"v_mul_f32 v142, v64, v142\n"
"v_mul_f32 v143, v64, v143\n"
"v_cvt_pk_bf16_f32 v156, v136, v137\n"
"v_cvt_pk_bf16_f32 v157, v138, v139\n"
"v_cvt_pk_bf16_f32 v158, v140, v141\n"
"v_cvt_pk_bf16_f32 v159, v142, v143\n"
"buffer_store_dwordx4 v[156:159], v251, s[52:55], s25 offen offset:3072 sc1\n"
"v_accvgpr_read_b32 v128, a4\n"
"v_accvgpr_read_b32 v129, a5\n"
"v_accvgpr_read_b32 v130, a6\n"
"v_accvgpr_read_b32 v131, a7\n"
"v_accvgpr_read_b32 v132, a20\n"
"v_accvgpr_read_b32 v133, a21\n"
"v_accvgpr_read_b32 v134, a22\n"
"v_accvgpr_read_b32 v135, a23\n"
"v_mul_f32 v128, v65, v128\n"
"v_mul_f32 v129, v65, v129\n"
"v_mul_f32 v130, v65, v130\n"
"v_mul_f32 v131, v65, v131\n"
"v_mul_f32 v132, v65, v132\n"
"v_mul_f32 v133, v65, v133\n"
"v_mul_f32 v134, v65, v134\n"
"v_mul_f32 v135, v65, v135\n"
"v_cvt_pk_bf16_f32 v144, v128, v129\n"
"v_cvt_pk_bf16_f32 v145, v130, v131\n"
"v_cvt_pk_bf16_f32 v146, v132, v133\n"
"v_cvt_pk_bf16_f32 v147, v134, v135\n"
"buffer_store_dwordx4 v[144:147], v251, s[52:55], s26 offen sc1\n"
"v_accvgpr_read_b32 v136, a36\n"
"v_accvgpr_read_b32 v137, a37\n"
"v_accvgpr_read_b32 v138, a38\n"
"v_accvgpr_read_b32 v139, a39\n"
"v_accvgpr_read_b32 v140, a52\n"
"v_accvgpr_read_b32 v141, a53\n"
"v_accvgpr_read_b32 v142, a54\n"
"v_accvgpr_read_b32 v143, a55\n"
"v_mul_f32 v136, v65, v136\n"
"v_mul_f32 v137, v65, v137\n"
"v_mul_f32 v138, v65, v138\n"
"v_mul_f32 v139, v65, v139\n"
"v_mul_f32 v140, v65, v140\n"
"v_mul_f32 v141, v65, v141\n"
"v_mul_f32 v142, v65, v142\n"
"v_mul_f32 v143, v65, v143\n"
"v_cvt_pk_bf16_f32 v148, v136, v137\n"
"v_cvt_pk_bf16_f32 v149, v138, v139\n"
"v_cvt_pk_bf16_f32 v150, v140, v141\n"
"v_cvt_pk_bf16_f32 v151, v142, v143\n"
"buffer_store_dwordx4 v[148:151], v251, s[52:55], s26 offen offset:1024 sc1\n"
"v_accvgpr_read_b32 v128, a68\n"
"v_accvgpr_read_b32 v129, a69\n"
"v_accvgpr_read_b32 v130, a70\n"
"v_accvgpr_read_b32 v131, a71\n"
"v_accvgpr_read_b32 v132, a84\n"
"v_accvgpr_read_b32 v133, a85\n"
"v_accvgpr_read_b32 v134, a86\n"
"v_accvgpr_read_b32 v135, a87\n"
"v_mul_f32 v128, v65, v128\n"
"v_mul_f32 v129, v65, v129\n"
"v_mul_f32 v130, v65, v130\n"
"v_mul_f32 v131, v65, v131\n"
"v_mul_f32 v132, v65, v132\n"
"v_mul_f32 v133, v65, v133\n"
"v_mul_f32 v134, v65, v134\n"
"v_mul_f32 v135, v65, v135\n"
"v_cvt_pk_bf16_f32 v152, v128, v129\n"
"v_cvt_pk_bf16_f32 v153, v130, v131\n"
"v_cvt_pk_bf16_f32 v154, v132, v133\n"
"v_cvt_pk_bf16_f32 v155, v134, v135\n"
"buffer_store_dwordx4 v[152:155], v251, s[52:55], s26 offen offset:2048 sc1\n"
"v_accvgpr_read_b32 v136, a100\n"
"v_accvgpr_read_b32 v137, a101\n"
"v_accvgpr_read_b32 v138, a102\n"
"v_accvgpr_read_b32 v139, a103\n"
"v_accvgpr_read_b32 v140, a116\n"
"v_accvgpr_read_b32 v141, a117\n"
"v_accvgpr_read_b32 v142, a118\n"
"v_accvgpr_read_b32 v143, a119\n"
"v_mul_f32 v136, v65, v136\n"
"v_mul_f32 v137, v65, v137\n"
"v_mul_f32 v138, v65, v138\n"
"v_mul_f32 v139, v65, v139\n"
"v_mul_f32 v140, v65, v140\n"
"v_mul_f32 v141, v65, v141\n"
"v_mul_f32 v142, v65, v142\n"
"v_mul_f32 v143, v65, v143\n"
"v_cvt_pk_bf16_f32 v156, v136, v137\n"
"v_cvt_pk_bf16_f32 v157, v138, v139\n"
"v_cvt_pk_bf16_f32 v158, v140, v141\n"
"v_cvt_pk_bf16_f32 v159, v142, v143\n"
"buffer_store_dwordx4 v[156:159], v251, s[52:55], s26 offen offset:3072 sc1\n"
"v_accvgpr_read_b32 v128, a8\n"
"v_accvgpr_read_b32 v129, a9\n"
"v_accvgpr_read_b32 v130, a10\n"
"v_accvgpr_read_b32 v131, a11\n"
"v_accvgpr_read_b32 v132, a24\n"
"v_accvgpr_read_b32 v133, a25\n"
"v_accvgpr_read_b32 v134, a26\n"
"v_accvgpr_read_b32 v135, a27\n"
"v_mul_f32 v128, v66, v128\n"
"v_mul_f32 v129, v66, v129\n"
"v_mul_f32 v130, v66, v130\n"
"v_mul_f32 v131, v66, v131\n"
"v_mul_f32 v132, v66, v132\n"
"v_mul_f32 v133, v66, v133\n"
"v_mul_f32 v134, v66, v134\n"
"v_mul_f32 v135, v66, v135\n"
"v_cvt_pk_bf16_f32 v144, v128, v129\n"
"v_cvt_pk_bf16_f32 v145, v130, v131\n"
"v_cvt_pk_bf16_f32 v146, v132, v133\n"
"v_cvt_pk_bf16_f32 v147, v134, v135\n"
"buffer_store_dwordx4 v[144:147], v251, s[52:55], s22 offen sc1\n"
"v_accvgpr_read_b32 v136, a40\n"
"v_accvgpr_read_b32 v137, a41\n"
"v_accvgpr_read_b32 v138, a42\n"
"v_accvgpr_read_b32 v139, a43\n"
"v_accvgpr_read_b32 v140, a56\n"
"v_accvgpr_read_b32 v141, a57\n"
"v_accvgpr_read_b32 v142, a58\n"
"v_accvgpr_read_b32 v143, a59\n"
"v_mul_f32 v136, v66, v136\n"
"v_mul_f32 v137, v66, v137\n"
"v_mul_f32 v138, v66, v138\n"
"v_mul_f32 v139, v66, v139\n"
"v_mul_f32 v140, v66, v140\n"
"v_mul_f32 v141, v66, v141\n"
"v_mul_f32 v142, v66, v142\n"
"v_mul_f32 v143, v66, v143\n"
"v_cvt_pk_bf16_f32 v148, v136, v137\n"
"v_cvt_pk_bf16_f32 v149, v138, v139\n"
"v_cvt_pk_bf16_f32 v150, v140, v141\n"
"v_cvt_pk_bf16_f32 v151, v142, v143\n"
"buffer_store_dwordx4 v[148:151], v251, s[52:55], s22 offen offset:1024 sc1\n"
"v_accvgpr_read_b32 v128, a72\n"
"v_accvgpr_read_b32 v129, a73\n"
"v_accvgpr_read_b32 v130, a74\n"
"v_accvgpr_read_b32 v131, a75\n"
"v_accvgpr_read_b32 v132, a88\n"
"v_accvgpr_read_b32 v133, a89\n"
"v_accvgpr_read_b32 v134, a90\n"
"v_accvgpr_read_b32 v135, a91\n"
"v_mul_f32 v128, v66, v128\n"
"v_mul_f32 v129, v66, v129\n"
"v_mul_f32 v130, v66, v130\n"
"v_mul_f32 v131, v66, v131\n"
"v_mul_f32 v132, v66, v132\n"
"v_mul_f32 v133, v66, v133\n"
"v_mul_f32 v134, v66, v134\n"
"v_mul_f32 v135, v66, v135\n"
"v_cvt_pk_bf16_f32 v152, v128, v129\n"
"v_cvt_pk_bf16_f32 v153, v130, v131\n"
"v_cvt_pk_bf16_f32 v154, v132, v133\n"
"v_cvt_pk_bf16_f32 v155, v134, v135\n"
"buffer_store_dwordx4 v[152:155], v251, s[52:55], s22 offen offset:2048 sc1\n"
"v_accvgpr_read_b32 v136, a104\n"
"v_accvgpr_read_b32 v137, a105\n"
"v_accvgpr_read_b32 v138, a106\n"
"v_accvgpr_read_b32 v139, a107\n"
"v_accvgpr_read_b32 v140, a120\n"
"v_accvgpr_read_b32 v141, a121\n"
"v_accvgpr_read_b32 v142, a122\n"
"v_accvgpr_read_b32 v143, a123\n"
"v_mul_f32 v136, v66, v136\n"
"v_mul_f32 v137, v66, v137\n"
"v_mul_f32 v138, v66, v138\n"
"v_mul_f32 v139, v66, v139\n"
"v_mul_f32 v140, v66, v140\n"
"v_mul_f32 v141, v66, v141\n"
"v_mul_f32 v142, v66, v142\n"
"v_mul_f32 v143, v66, v143\n"
"v_cvt_pk_bf16_f32 v156, v136, v137\n"
"v_cvt_pk_bf16_f32 v157, v138, v139\n"
"v_cvt_pk_bf16_f32 v158, v140, v141\n"
"v_cvt_pk_bf16_f32 v159, v142, v143\n"
"buffer_store_dwordx4 v[156:159], v251, s[52:55], s22 offen offset:3072 sc1\n"
"v_accvgpr_read_b32 v128, a12\n"
"v_accvgpr_read_b32 v129, a13\n"
"v_accvgpr_read_b32 v130, a14\n"
"v_accvgpr_read_b32 v131, a15\n"
"v_accvgpr_read_b32 v132, a28\n"
"v_accvgpr_read_b32 v133, a29\n"
"v_accvgpr_read_b32 v134, a30\n"
"v_accvgpr_read_b32 v135, a31\n"
"v_mul_f32 v128, v67, v128\n"
"v_mul_f32 v129, v67, v129\n"
"v_mul_f32 v130, v67, v130\n"
"v_mul_f32 v131, v67, v131\n"
"v_mul_f32 v132, v67, v132\n"
"v_mul_f32 v133, v67, v133\n"
"v_mul_f32 v134, v67, v134\n"
"v_mul_f32 v135, v67, v135\n"
"v_cvt_pk_bf16_f32 v144, v128, v129\n"
"v_cvt_pk_bf16_f32 v145, v130, v131\n"
"v_cvt_pk_bf16_f32 v146, v132, v133\n"
"v_cvt_pk_bf16_f32 v147, v134, v135\n"
"buffer_store_dwordx4 v[144:147], v251, s[52:55], s23 offen sc1\n"
"v_accvgpr_read_b32 v136, a44\n"
"v_accvgpr_read_b32 v137, a45\n"
"v_accvgpr_read_b32 v138, a46\n"
"v_accvgpr_read_b32 v139, a47\n"
"v_accvgpr_read_b32 v140, a60\n"
"v_accvgpr_read_b32 v141, a61\n"
"v_accvgpr_read_b32 v142, a62\n"
"v_accvgpr_read_b32 v143, a63\n"
"v_mul_f32 v136, v67, v136\n"
"v_mul_f32 v137, v67, v137\n"
"v_mul_f32 v138, v67, v138\n"
"v_mul_f32 v139, v67, v139\n"
"v_mul_f32 v140, v67, v140\n"
"v_mul_f32 v141, v67, v141\n"
"v_mul_f32 v142, v67, v142\n"
"v_mul_f32 v143, v67, v143\n"
"v_cvt_pk_bf16_f32 v148, v136, v137\n"
"v_cvt_pk_bf16_f32 v149, v138, v139\n"
"v_cvt_pk_bf16_f32 v150, v140, v141\n"
"v_cvt_pk_bf16_f32 v151, v142, v143\n"
"buffer_store_dwordx4 v[148:151], v251, s[52:55], s23 offen offset:1024 sc1\n"
"v_accvgpr_read_b32 v128, a76\n"
"v_accvgpr_read_b32 v129, a77\n"
"v_accvgpr_read_b32 v130, a78\n"
"v_accvgpr_read_b32 v131, a79\n"
"v_accvgpr_read_b32 v132, a92\n"
"v_accvgpr_read_b32 v133, a93\n"
"v_accvgpr_read_b32 v134, a94\n"
"v_accvgpr_read_b32 v135, a95\n"
"v_mul_f32 v128, v67, v128\n"
"v_mul_f32 v129, v67, v129\n"
"v_mul_f32 v130, v67, v130\n"
"v_mul_f32 v131, v67, v131\n"
"v_mul_f32 v132, v67, v132\n"
"v_mul_f32 v133, v67, v133\n"
"v_mul_f32 v134, v67, v134\n"
"v_mul_f32 v135, v67, v135\n"
"v_cvt_pk_bf16_f32 v152, v128, v129\n"
"v_cvt_pk_bf16_f32 v153, v130, v131\n"
"v_cvt_pk_bf16_f32 v154, v132, v133\n"
"v_cvt_pk_bf16_f32 v155, v134, v135\n"
"buffer_store_dwordx4 v[152:155], v251, s[52:55], s23 offen offset:2048 sc1\n"
"v_accvgpr_read_b32 v136, a108\n"
"v_accvgpr_read_b32 v137, a109\n"
"v_accvgpr_read_b32 v138, a110\n"
"v_accvgpr_read_b32 v139, a111\n"
"v_accvgpr_read_b32 v140, a124\n"
"v_accvgpr_read_b32 v141, a125\n"
"v_accvgpr_read_b32 v142, a126\n"
"v_accvgpr_read_b32 v143, a127\n"
"v_mul_f32 v136, v67, v136\n"
"v_mul_f32 v137, v67, v137\n"
"v_mul_f32 v138, v67, v138\n"
"v_mul_f32 v139, v67, v139\n"
"v_mul_f32 v140, v67, v140\n"
"v_mul_f32 v141, v67, v141\n"
"v_mul_f32 v142, v67, v142\n"
"v_mul_f32 v143, v67, v143\n"
"v_cvt_pk_bf16_f32 v156, v136, v137\n"
"v_cvt_pk_bf16_f32 v157, v138, v139\n"
"v_cvt_pk_bf16_f32 v158, v140, v141\n"
"v_cvt_pk_bf16_f32 v159, v142, v143\n"
"buffer_store_dwordx4 v[156:159], v251, s[52:55], s23 offen offset:3072 sc1\n"
"s_waitcnt vmcnt(0)\n"
"s_branch L_pub_%=\n"
"L_end_%=:\n"
  :: "s"(__builtin_amdgcn_kernarg_segment_ptr()), "s"(blockIdx.x), "s"(wid) : "v0","v1","v2","v3","v4","v5","v6","v7","v8","v9","v10","v11","v12","v13","v14","v15","v16","v17","v18","v19","v20","v21","v22","v23","v24","v25","v26","v27","v28","v29","v30","v31","v32","v33","v34","v35","v36","v37","v38","v39","v40","v41","v42","v43","v44","v45","v46","v47","v48","v49","v50","v51","v52","v53","v54","v55","v56","v57","v58","v59","v60","v61","v62","v63","v64","v65","v66","v67","v68","v69","v70","v71","v72","v73","v74","v75","v76","v77","v78","v79","v80","v81","v82","v83","v84","v85","v86","v87","v88","v89","v90","v91","v92","v93","v94","v95","v96","v97","v98","v99","v100","v101","v102","v103","v104","v105","v106","v107","v108","v109","v110","v111","v112","v113","v114","v115","v116","v117","v118","v119","v120","v121","v122","v123","v124","v125","v126","v127","v128","v129","v130","v131","v132","v133","v134","v135","v136","v137","v138","v139","v140","v141","v142","v143","v144","v145","v146","v147","v148","v149","v150","v151","v152","v153","v154","v155","v156","v157","v158","v159","v160","v161","v162","v163","v164","v165","v166","v167","v168","v169","v170","v171","v172","v173","v174","v175","v176","v177","v178","v179","v180","v181","v182","v183","v184","v185","v186","v187","v188","v189","v190","v191","v192","v193","v194","v195","v196","v197","v198","v199","v200","v201","v202","v203","v204","v205","v206","v207","v208","v209","v210","v211","v212","v213","v214","v215","v216","v217","v218","v219","v220","v221","v222","v223","v224","v225","v226","v227","v228","v229","v230","v231","v232","v233","v234","v235","v236","v237","v238","v239","v240","v241","v242","v243","v244","v245","v246","v247","v248","v249","v250","v251","v252","v253","v254","v255","a0","a1","a2","a3","a4","a5","a6","a7","a8","a9","a10","a11","a12","a13","a14","a15","a16","a17","a18","a19","a20","a21","a22","a23","a24","a25","a26","a27","a28","a29","a30","a31","a32","a33","a34","a35","a36","a37","a38","a39","a40","a41","a42","a43","a44","a45","a46","a47","a48","a49","a50","a51","a52","a53","a54","a55","a56","a57","a58","a59","a60","a61","a62","a63","a64","a65","a66","a67","a68","a69","a70","a71","a72","a73","a74","a75","a76","a77","a78","a79","a80","a81","a82","a83","a84","a85","a86","a87","a88","a89","a90","a91","a92","a93","a94","a95","a96","a97","a98","a99","a100","a101","a102","a103","a104","a105","a106","a107","a108","a109","a110","a111","a112","a113","a114","a115","a116","a117","a118","a119","a120","a121","a122","a123","a124","a125","a126","a127","a128","a129","a130","a131","a132","a133","a134","a135","a136","a137","a138","a139","a140","a141","a142","a143","a144","a145","a146","a147","a148","a149","a150","a151","a152","a153","a154","a155","a156","a157","a158","a159","a160","a161","a162","a163","a164","a165","a166","a167","a168","a169","a170","a171","a172","a173","a174","a175","a176","a177","a178","a179","a180","a181","a182","a183","a184","a185","a186","a187","a188","a189","a190","a191","a192","a193","a194","a195","a196","a197","a198","a199","a200","a201","a202","a203","a204","a205","a206","a207","a208","a209","a210","a211","a212","a213","a214","a215","a216","a217","a218","a219","a220","a221","a222","a223","a224","a225","a226","a227","a228","a229","a230","a231","a232","a233","a234","a235","a236","a237","a238","a239","a240","a241","a242","a243","a244","a245","a246","a247","a248","a249","a250","a251","a252","a253","a254","a255","s4","s5","s6","s7","s8","s9","s10","s11","s12","s13","s14","s15","s16","s17","s18","s19","s20","s21","s22","s23","s24","s25","s26","s27","s28","s29","s30","s31","s33","s34","s35","s36","s37","s38","s39","s40","s41","s42","s43","s44","s45","s46","s47","s48","s49","s50","s51","s52","s53","s54","s55","s56","s57","s58","s59","s60","s61","s62","s63","s64","s65","s66","s67","s68","s69","s70","s71","s72","s73","s74","s75","s76","s77","s78","s79","s80","s81","s82","s83","vcc","scc","memory");
}

extern "C" void kernel_launch(void* const* d_in, const int* in_sizes, int n_in, void* d_out, int out_size, void* d_ws, size_t ws_size, hipStream_t stream) {
  mha_main<<<256, 256, 0, stream>>>(d_in[0], d_in[1], d_in[2], d_in[3], d_in[4], d_out, d_ws);
}
```
